```python
import torch

def _softmax(x, axis=-1):
    return torch.softmax(x, dim=axis)

def _gelu(x):
    return torch.nn.functional.gelu(x, approximate='tanh')

def _silu(x):
    return torch.nn.functional.silu(x)

def _sigmoid(x):
    return torch.sigmoid(x)

def _layernorm(x, axis=-1, eps=1e-05):
    m = x.mean(dim=axis, keepdim=True)
    v = x.var(dim=axis, keepdim=True, unbiased=False)
    return (x - m) / torch.sqrt(v + eps)

def _scatter_add(tgt, idx, src, axis=0):
    return tgt.clone().index_add_(axis, idx, src)

def _segment_sum(src, seg, n):
    out = torch.zeros((n,) + src.shape[1:], dtype=src.dtype)
    return out.index_add_(0, seg, src)

def _pad(x, pw):
    flat = tuple((v for pair in reversed(pw) for v in pair))
    return torch.nn.functional.pad(x, flat)
kernel_name = 'standalone__autoregressive_conv_wavenet__speech'

def setup_inputs():

    _w = lambda a: (lambda t: t.to(torch.bfloat16) if t.is_floating_point() else t)(torch.as_tensor(a))
    B, T, D, K = (2, 1000, 512, 3)
    return {'x': _w((lambda *s: torch.randn(*s, dtype=torch.bfloat16) * 0.1)(B, T, D)), 'w': _w((lambda *s: torch.randn(*s, dtype=torch.bfloat16) * 0.1)(K, D, 2 * D)), 'w_o': _w((lambda *s: torch.randn(*s, dtype=torch.bfloat16) * 0.05)(D, D))}

def reference(x, w, w_o):
    B, T, D = x.shape
    K = w.shape[0]
    dil = 2
    pad = (K - 1) * dil
    xp = _pad(x, ((0, 0), (pad, 0), (0, 0)))
    h = torch.einsum('btc,cd->btd', xp[:, pad:pad + T, :], w[0])
    for i in range(1, K):
        off = pad - i * dil
        h = h + torch.einsum('btc,cd->btd', xp[:, off:off + T, :], w[i])
    f = h[..., :D].clone()
    g = h[..., D:].clone()
    z = torch.tanh(f) * _sigmoid(g)
    return z @ w_o
```

```cpp
#include <hip/hip_runtime.h>
#include <stdint.h>

typedef short s16x4 __attribute__((ext_vector_type(4)));
typedef __bf16 bf16x8 __attribute__((ext_vector_type(8)));
typedef __bf16 bf16x2 __attribute__((ext_vector_type(2)));
typedef float f32x16 __attribute__((ext_vector_type(16)));
typedef float f32x2 __attribute__((ext_vector_type(2)));
typedef int i32x4 __attribute__((ext_vector_type(4)));
typedef unsigned u32x4 __attribute__((ext_vector_type(4)));
typedef __attribute__((address_space(3))) s16x4* lds_s16x4;
typedef __attribute__((address_space(3))) bf16x8* lds_bf16x8;

#define RING 5
#define GM 16
#define ROT 1
constexpr int T_LEN = 1000, DM = 512;
constexpr int WSLOT = 24576;
constexpr int XSLOT = 68 * 128;
constexpr int XRING = (RING + 2) / 2;
constexpr int XOFF = 0;
constexpr int WOFF = XRING * XSLOT > 0 ? ((XRING * XSLOT + 255) / 256) * 256 : 0;
constexpr int LDS1 = WOFF + RING * WSLOT;
constexpr int NS = 16;

__device__ __forceinline__ i32x4 make_srd(const void* p, unsigned bytes) {
  uint64_t a = (uint64_t)p;
  i32x4 r;
  r[0] = (int)(unsigned)a;
  r[1] = (int)(unsigned)(a >> 32);
  r[2] = (int)bytes;
  r[3] = 0x00020000;
  return r;
}

__device__ __forceinline__ void dma16(i32x4 srd, unsigned voff, unsigned soff, unsigned ldsaddr) {
  asm volatile("s_mov_b32 m0, %3\n\ts_nop 0\n\tbuffer_load_dwordx4 %0, %1, %2 offen lds"
               :: "v"(voff), "s"(srd), "s"(soff), "s"(ldsaddr));
}
#define WAITV(n) asm volatile("s_waitcnt vmcnt(" #n ")" ::: "memory")
#define BARRIER() asm volatile("s_waitcnt lgkmcnt(0)\n\ts_barrier" ::: "memory")

__device__ __forceinline__ void waitv_dyn(int n) {
  switch (n) {
    case 0: WAITV(0); break;
    case 6: WAITV(6); break;
    case 9: WAITV(9); break;
    case 15: WAITV(15); break;
    case 21: WAITV(21); break;
    case 24: WAITV(24); break;
    case 30: WAITV(30); break;
    default: WAITV(0); break;
  }
}

__device__ __forceinline__ unsigned pack_bf16(float a, float b) {
  f32x2 v = {a, b};
  bf16x2 r = __builtin_convertvector(v, bf16x2);
  return __builtin_bit_cast(unsigned, r);
}

__device__ __forceinline__ float gate(float f, float g) {
  const float L2E = 1.4426950408889634f;
  float u = __builtin_amdgcn_exp2f(fminf(f * (-2.0f * L2E), 60.0f));
  float b = __builtin_amdgcn_exp2f(g * (-L2E));
  float t1 = 1.0f + u;
  float den = __builtin_fmaf(t1, b, t1);
  return (2.0f - t1) * __builtin_amdgcn_rcpf(den);
}

struct Frags { bf16x8 x[4]; bf16x8 w[3]; };

__global__ __launch_bounds__(256, 1) void wn_gemm1(const uint16_t* __restrict__ x, const uint16_t* __restrict__ w,
                                                   u32x4* __restrict__ zws) {
  extern __shared__ __attribute__((aligned(256))) char smem[];
  const int tid = threadIdx.x, lane = tid & 63;
  const int wv = __builtin_amdgcn_readfirstlane(tid >> 6);
  const int bid = blockIdx.x, xcd = bid & 7, j = bid >> 3;
  constexpr int GN = 32 / GM, NGM = 32 / GM;
  const int mi = (xcd % NGM) * GM + (j % GM);
  const int ni = (xcd / NGM) * GN + (j / GM);
  const int b = mi >> 4, t0 = (mi & 15) * 64;
  const int rot = ROT ? (((j % GM) * (GM >= 8 ? 1 : 8 / GM) + (j / GM) * (8 / (GN > 8 ? 8 : GN))) & 7) : 0;

  const i32x4 srdx = make_srd(x, 2 * T_LEN * DM * 2);
  const i32x4 srdw = make_srd(w, 3 * DM * 2 * DM * 2);
  const unsigned sbase = (unsigned)(uintptr_t)(__attribute__((address_space(3))) char*)smem;

  const unsigned wvoff = (unsigned)((lane >> 4) * 2048 + ((lane >> 3) & 1) * 1024 + ni * 128 + (((lane & 7) ^ (2 * (lane >> 4))) * 16));
  unsigned xvoff[3];
#pragma unroll
  for (int i = 0; i < 3; ++i) {
    int rho = i < 2 ? 16 * wv + 8 * i + (lane >> 3) : 64 + wv;
    int t = t0 - 4 + rho;
    int tc = t > T_LEN - 1 ? T_LEN - 1 : t;
    unsigned off = (unsigned)((b * T_LEN + tc) * 1024 + (((lane & 7) ^ ((rho >> 2) & 7)) * 16));
    xvoff[i] = t < 0 ? 0x80000000u : off;
  }
  auto issue = [&](int s) {
    const int cc = ((s >> 1) + rot) & 7;
    const int c0 = cc * 64 + (s & 1) * 32;
    const unsigned wl = sbase + WOFF + (s % RING) * WSLOT;
    if ((s & 1) == 0) {
      const unsigned xl = sbase + XOFF + ((s >> 1) % XRING) * XSLOT;
      dma16(srdx, xvoff[0], cc * 128, xl + (16 * wv) * 128);
      dma16(srdx, xvoff[1], cc * 128, xl + (16 * wv + 8) * 128);
      if (lane < 8) dma16(srdx, xvoff[2], cc * 128, xl + (64 + wv) * 128);
    }
#pragma unroll
    for (int tp = 0; tp < 3; ++tp)
#pragma unroll
      for (int i = 0; i < 2; ++i)
        dma16(srdw, wvoff, (unsigned)((tp * 512 + c0 + 8 * wv + 4 * i) * 2048), wl + tp * 8192 + (8 * wv + 4 * i) * 256);
  };

  const int G = lane >> 4, q4 = (lane >> 2) & 3, p4 = lane & 3;
  const int pperm = ((p4 & 1) << 1) | (p4 >> 1);
  const unsigned wrd = (unsigned)((8 * (G >> 1) + q4) * 256 + (G & 1) * 128 + ((wv ^ q4) * 32) + pperm * 8);
  const int c32 = lane & 31, h = lane >> 5, xq = c32 >> 1, xe = c32 & 1;
  const unsigned xrd0 = (unsigned)((4 * xq + xe) * 128 + ((h ^ (xq & 7)) * 16));
  const unsigned xrd1 = (unsigned)((4 * (xq + 1) + xe) * 128 + ((h ^ ((xq + 1) & 7)) * 16));

  auto load_frags = [&](Frags& F, int s, int ks) {
    const char* wp = smem + WOFF + (s % RING) * WSLOT + wrd + ks * 16 * 256;
    const char* xp = smem + XOFF + ((s >> 1) % XRING) * XSLOT;
    const unsigned kx = (unsigned)(((s & 1) * 2 + ks) * 32);
    F.x[0] = *(const bf16x8*)(xp + (xrd0 ^ kx));
    F.x[1] = *(const bf16x8*)(xp + (xrd0 ^ kx) + 256);
    F.x[2] = *(const bf16x8*)(xp + (xrd1 ^ kx));
    F.x[3] = *(const bf16x8*)(xp + (xrd1 ^ kx) + 256);
#pragma unroll
    for (int tp = 0; tp < 3; ++tp) {
      s16x4 lo = __builtin_amdgcn_ds_read_tr16_b64_v4i16((lds_s16x4)(wp + tp * 8192));
      s16x4 hi = __builtin_amdgcn_ds_read_tr16_b64_v4i16((lds_s16x4)(wp + tp * 8192 + 4 * 256));
      typedef short s16x8 __attribute__((ext_vector_type(8)));
      s16x8 v = __builtin_shufflevector(lo, hi, 0, 1, 2, 3, 4, 5, 6, 7);
      F.w[tp] = __builtin_bit_cast(bf16x8, v);
    }
  };
  f32x16 acc0 = {0}, acc1 = {0};
  auto mma = [&](const Frags& F) {
#pragma unroll
    for (int tp = 0; tp < 3; ++tp) {
      acc0 = __builtin_amdgcn_mfma_f32_32x32x16_bf16(F.w[tp], F.x[2 - tp], acc0, 0, 0, 0);
      acc1 = __builtin_amdgcn_mfma_f32_32x32x16_bf16(F.w[tp], F.x[3 - tp], acc1, 0, 0, 0);
    }
  };
  auto later = [&](int s) {
    int n = 0;
#pragma unroll
    for (int d = 1; d <= RING - 2; ++d) { int u = s + d; if (u < NS) n += (u & 1) ? 6 : 9; }
    return n;
  };

#pragma unroll
  for (int s = 0; s < RING - 1; ++s) issue(s);

  Frags A, B;
  waitv_dyn(later(0));
  BARRIER();
  issue(RING - 1);
  load_frags(A, 0, 0);
  load_frags(B, 0, 1);
  mma(A);
#pragma unroll 1
  for (int s = 1; s < NS; ++s) {
    waitv_dyn(later(s));
    BARRIER();
    if (s + RING - 1 < NS) issue(s + RING - 1);
    load_frags(A, s, 0);
    mma(B);
    load_frags(B, s, 1);
    mma(A);
  }
  mma(B);

  const int kg = ni * 4 + wv;
#pragma unroll
  for (int m = 0; m < 2; ++m) {
    const f32x16& a = m ? acc1 : acc0;
    u32x4 o;
#pragma unroll
    for (int r = 0; r < 4; ++r) o[r] = pack_bf16(gate(a[2 * r], a[2 * r + 8]), gate(a[2 * r + 1], a[2 * r + 9]));
    zws[((mi * 2 + m) * 32 + kg) * 64 + lane] = o;
  }
}

constexpr int LDS2 = 512 * 128;
__global__ __launch_bounds__(256, 1) void wn_gemm2(const u32x4* __restrict__ zws, const uint16_t* __restrict__ wo,
                                                   uint16_t* __restrict__ out) {
  extern __shared__ __attribute__((aligned(256))) char smem[];
  const int tid = threadIdx.x, lane = tid & 63;
  const int wv = __builtin_amdgcn_readfirstlane(tid >> 6);
  const int bid = blockIdx.x, xcd = bid & 7, j = bid >> 3;
  const int mi = xcd * 4 + (j & 3), cj = j >> 2;
  const int b = mi >> 4, t0 = (mi & 15) * 64;
  const int tf = wv & 1, ch = wv >> 1;
  const i32x4 srd = make_srd(wo, DM * DM * 2);
  const unsigned sbase = (unsigned)(uintptr_t)(__attribute__((address_space(3))) char*)smem;
  const unsigned voff = (unsigned)((lane >> 3) * 1024 + cj * 128 + (((lane & 7) ^ (4 * ((lane >> 4) & 1))) * 16));
#pragma unroll
  for (int i = 0; i < 16; ++i) {
    const int k0 = (i * 4 + wv) * 8;
    dma16(srd, voff, (unsigned)(k0 * 1024), sbase + k0 * 128);
  }
  bf16x8 z[32];
  const u32x4* zp = zws + ((mi * 2 + tf) * 32) * 64 + lane;
#pragma unroll
  for (int k = 0; k < 32; ++k) z[k] = __builtin_bit_cast(bf16x8, zp[k * 64]);
  WAITV(0);
  BARRIER();
  const int G = lane >> 4, q4 = (lane >> 2) & 3, p4 = lane & 3;
  const int cq = 4 * (p4 & 1) + 2 * (G & 1) + (p4 >> 1);
  const unsigned ard = (unsigned)((8 * (G >> 1) + q4) * 128 + ((ch ^ ((q4 >> 1) & 1)) * 64) + cq * 8);
  f32x16 acc = {0};
#pragma unroll
  for (int k = 0; k < 32; ++k) {
    const char* ap = smem + ard + k * 16 * 128;
    s16x4 lo = __builtin_amdgcn_ds_read_tr16_b64_v4i16((lds_s16x4)(ap));
    s16x4 hi = __builtin_amdgcn_ds_read_tr16_b64_v4i16((lds_s16x4)(ap + 4 * 128));
    typedef short s16x8 __attribute__((ext_vector_type(8)));
    s16x8 v = __builtin_shufflevector(lo, hi, 0, 1, 2, 3, 4, 5, 6, 7);
    acc = __builtin_amdgcn_mfma_f32_32x32x16_bf16(__builtin_bit_cast(bf16x8, v), z[k], acc, 0, 0, 0);
  }
  const int c32 = lane & 31, h = lane >> 5;
  const int t = t0 + 4 * (c32 >> 1) + (c32 & 1) + 2 * tf;
  if (t < T_LEN) {
    u32x4 o0, o1;
#pragma unroll
    for (int r = 0; r < 4; ++r) {
      o0[r] = pack_bf16(acc[2 * r], acc[2 * r + 1]);
      o1[r] = pack_bf16(acc[8 + 2 * r], acc[8 + 2 * r + 1]);
    }
    u32x4* op = (u32x4*)(out + (size_t)(b * T_LEN + t) * DM + cj * 64 + ch * 32 + h * 16);
    op[0] = o0;
    op[1] = o1;
  }
}

extern "C" void kernel_launch(void* const* d_in, const int* in_sizes, int n_in, void* d_out, int out_size,
                              void* d_ws, size_t ws_size, hipStream_t stream) {
  const uint16_t* x = (const uint16_t*)d_in[0];
  const uint16_t* w = (const uint16_t*)d_in[1];
  const uint16_t* wo = (const uint16_t*)d_in[2];
  (void)hipFuncSetAttribute((const void*)wn_gemm1, hipFuncAttributeMaxDynamicSharedMemorySize, LDS1);
  (void)hipFuncSetAttribute((const void*)wn_gemm2, hipFuncAttributeMaxDynamicSharedMemorySize, LDS2);
  wn_gemm1<<<256, 256, LDS1, stream>>>(x, w, (u32x4*)d_ws);
  wn_gemm2<<<256, 256, LDS2, stream>>>((const u32x4*)d_ws, wo, (uint16_t*)d_out);
}
```

```cpp
#include <hip/hip_runtime.h>
#include <stdint.h>
#include <type_traits>

typedef short s16x4 __attribute__((ext_vector_type(4)));
typedef short s16x8 __attribute__((ext_vector_type(8)));
typedef __bf16 bf16x8 __attribute__((ext_vector_type(8)));
typedef __bf16 bf16x2 __attribute__((ext_vector_type(2)));
typedef float f32x16 __attribute__((ext_vector_type(16)));
typedef float f32x2 __attribute__((ext_vector_type(2)));
typedef int i32x4 __attribute__((ext_vector_type(4)));
typedef unsigned u32x4 __attribute__((ext_vector_type(4)));
typedef __attribute__((address_space(3))) s16x4* lds_s16x4;

#define RING 5
#define GM 16
#define ROT 1
#define PROF_LDS 0
#define STAMP(i)
#define CSTAMP(i)
#define STAMP_INIT()
#define STAMP_DUMP(k,base)

constexpr int T_LEN = 1000, DM = 512;
constexpr int WSLOT = 24576;
constexpr int XSLOT = 68 * 128;
constexpr int XRING = (RING + 2) / 2;
constexpr int XOFF = 0;
constexpr int WOFF = ((XRING * XSLOT + 255) / 256) * 256;
constexpr int LDS1 = WOFF + RING * WSLOT;
constexpr int NS = 16;
constexpr int LDS2 = 512 * 128;

__device__ __forceinline__ i32x4 make_srd(const void* p, unsigned bytes) {
  uint64_t a = (uint64_t)p;
  i32x4 r;
  r[0] = (int)(unsigned)a;
  r[1] = (int)(unsigned)(a >> 32);
  r[2] = (int)bytes;
  r[3] = 0x00020000;
  return r;
}

__device__ __forceinline__ void dma16(i32x4 srd, unsigned voff, unsigned soff, unsigned ldsaddr) {
  asm volatile("s_mov_b32 m0, %3\n\ts_nop 0\n\tbuffer_load_dwordx4 %0, %1, %2 offen lds"
               :: "v"(voff), "s"(srd), "s"(soff), "s"(ldsaddr));
}
__device__ __forceinline__ void dma16_lo8(i32x4 srd, unsigned voff, unsigned soff, unsigned ldsaddr) {
  asm volatile("s_mov_b32 m0, %3\n\ts_mov_b64 exec, 0xff\n\tbuffer_load_dwordx4 %0, %1, %2 offen lds\n\ts_mov_b64 exec, -1"
               :: "v"(voff), "s"(srd), "s"(soff), "s"(ldsaddr));
}
template <int N> __device__ __forceinline__ void waitv() { asm volatile("s_waitcnt vmcnt(%0)" :: "i"(N) : "memory"); }
#define BARRIER() asm volatile("s_waitcnt lgkmcnt(0)\n\ts_barrier" ::: "memory")
#define SB() __builtin_amdgcn_sched_barrier(0)

constexpr int later(int s) {
  int n = 0;
  for (int d = 1; d <= RING - 2; ++d) { int u = s + d; if (u < NS) n += (u & 1) ? 6 : 9; }
  return n;
}

__device__ __forceinline__ unsigned pack_bf16(float a, float b) {
  f32x2 v = {a, b};
  bf16x2 r = __builtin_convertvector(v, bf16x2);
  return __builtin_bit_cast(unsigned, r);
}

__device__ __forceinline__ float gate(float f, float g) {
  const float L2E = 1.4426950408889634f;
  float u = __builtin_amdgcn_exp2f(fminf(f * (-2.0f * L2E), 60.0f));
  float b = __builtin_amdgcn_exp2f(g * (-L2E));
  float t1 = 1.0f + u;
  float den = __builtin_fmaf(t1, b, t1);
  return (2.0f - t1) * __builtin_amdgcn_rcpf(den);
}

__device__ __forceinline__ bf16x8 tr_frag(const char* p, int step) {
  s16x4 lo = __builtin_amdgcn_ds_read_tr16_b64_v4i16((lds_s16x4)(p));
  s16x4 hi = __builtin_amdgcn_ds_read_tr16_b64_v4i16((lds_s16x4)(p + step));
  s16x8 v = __builtin_shufflevector(lo, hi, 0, 1, 2, 3, 4, 5, 6, 7);
  return __builtin_bit_cast(bf16x8, v);
}

struct Frags { bf16x8 x0, x1, x2, x3, w0, w1, w2; };
#define MFMA(a, b, c) __builtin_amdgcn_mfma_f32_32x32x16_bf16(a, b, c, 0, 0, 0)

template <int S, int E, class F> __device__ __forceinline__ void static_for(F&& f) {
  if constexpr (S < E) { f(std::integral_constant<int, S>{}); static_for<S + 1, E>(f); }
}

__global__ __launch_bounds__(512, 2) void wn_gemm1(const uint16_t* __restrict__ x, const uint16_t* __restrict__ w,
                                                   u32x4* __restrict__ zws) {
  extern __shared__ __attribute__((aligned(256))) char smem[];
  constexpr int PROFOFF = LDS1; (void)PROFOFF;
  STAMP_INIT(); STAMP(0); CSTAMP(22);
  const int tid = threadIdx.x, lane = tid & 63;
  const int wv8 = __builtin_amdgcn_readfirstlane(tid >> 6);
  const int wv = wv8 & 3;
  const int bid = blockIdx.x, xcd = bid & 7, j = bid >> 3;
  constexpr int GN = 32 / GM, NGM = 32 / GM;
  const int mi = (xcd % NGM) * GM + (j % GM);
  const int ni = (xcd / NGM) * GN + (j / GM);
  const int b = mi >> 4, t0 = (mi & 15) * 64;

  if (wv8 >= 4) {
    const int rot = ROT ? (((j % GM) * (GM >= 8 ? 1 : 8 / GM) + (j / GM) * (8 / (GN > 8 ? 8 : GN))) & 7) : 0;
    const i32x4 srdx = make_srd(x, 2 * T_LEN * DM * 2);
    const i32x4 srdw = make_srd(w, 3 * DM * 2 * DM * 2);
    const unsigned sbase = (unsigned)(uintptr_t)(__attribute__((address_space(3))) char*)smem;
    const unsigned wvoff = (unsigned)((lane >> 4) * 2048 + ((lane >> 3) & 1) * 1024 + ni * 128 + (((lane & 7) ^ (2 * (lane >> 4))) * 16));
    unsigned xvoff[3];
#pragma unroll
    for (int i = 0; i < 3; ++i) {
      int rho = i < 2 ? 16 * wv + 8 * i + (lane >> 3) : 64 + wv;
      int t = t0 - 4 + rho;
      int tc = t > T_LEN - 1 ? T_LEN - 1 : t;
      unsigned off = (unsigned)((b * T_LEN + tc) * 1024 + (((lane & 7) ^ ((rho >> 2) & 7)) * 16));
      xvoff[i] = t < 0 ? 0x80000000u : off;
    }
    auto issue = [&](int s, bool even, int wslot, int xslot) {
      const int cc = ((s >> 1) + rot) & 7;
      const int c0 = cc * 64 + (even ? 0 : 32);
      const unsigned wl = sbase + WOFF + wslot * WSLOT + 8 * wv * 256;
      const unsigned so = (unsigned)(c0 + 8 * wv) * 2048;
      if (even) {
        const unsigned xl = sbase + XOFF + xslot * XSLOT;
        dma16(srdx, xvoff[0], cc * 128, xl + (16 * wv) * 128);
        dma16(srdx, xvoff[1], cc * 128, xl + (16 * wv + 8) * 128);
        dma16_lo8(srdx, xvoff[2], cc * 128, xl + (64 + wv) * 128);
      }
#pragma unroll
      for (int tp = 0; tp < 3; ++tp)
#pragma unroll
        for (int i = 0; i < 2; ++i)
          dma16(srdw, wvoff, so + (unsigned)((tp * 512 + 4 * i) * 2048), wl + tp * 8192 + 4 * i * 256);
    };
    static_for<0, RING - 1>([&](auto S) { constexpr int s = S; issue(s, (s & 1) == 0, s % RING, (s >> 1) % XRING); });
    constexpr int MAIN = ((NS - RING + 1) / 2) * 2;
    int ws = (RING - 1) % RING, xs = (((RING - 1) + 1) >> 1) % XRING;
#pragma unroll 1
    for (int s = 0; s < MAIN; s += 2) {
      waitv<later(0)>();
      BARRIER();
      {
        constexpr bool ev = ((RING - 1) & 1) == 0;
        issue(s + RING - 1, ev, ws, xs);
        ws = ws + 1 == RING ? 0 : ws + 1;
        if (ev) xs = xs + 1 == XRING ? 0 : xs + 1;
      }
      waitv<later(1)>();
      BARRIER();
      {
        constexpr bool ev = (RING & 1) == 0;
        issue(s + RING, ev, ws, xs);
        ws = ws + 1 == RING ? 0 : ws + 1;
        if (ev) xs = xs + 1 == XRING ? 0 : xs + 1;
      }
    }
    static_for<MAIN, NS>([&](auto S) {
      constexpr int s = S;
      waitv<later(s)>();
      BARRIER();
      if constexpr (s + RING - 1 < NS) issue(s + RING - 1, ((s + RING - 1) & 1) == 0, (s + RING - 1) % RING, ((s + RING - 1) >> 1) % XRING);
    });
    return;
  }

  const int G = lane >> 4, q4 = (lane >> 2) & 3, p4 = lane & 3;
  const int pperm = ((p4 & 1) << 1) | (p4 >> 1);
  const unsigned wrd = (unsigned)(WOFF + (8 * (G >> 1) + q4) * 256 + (G & 1) * 128 + ((wv ^ q4) * 32) + pperm * 8);
  const int c32 = lane & 31, h = lane >> 5, xq = c32 >> 1, xe = c32 & 1;
  const unsigned xrd0 = (unsigned)(XOFF + (4 * xq + xe) * 128 + ((h ^ (xq & 7)) * 16));
  const unsigned xrd1 = (unsigned)(XOFF + (4 * (xq + 1) + xe) * 128 + ((h ^ ((xq + 1) & 7)) * 16));

  f32x16 acc0 = {0}, acc1 = {0};
  auto half = [&](Frags& L, const Frags& U, const char* wp, const char* xa, const char* xb) {
#define LD(x) x
    acc0 = MFMA(U.w0, U.x2, acc0);
    LD(L.w0 = tr_frag(wp, 1024); L.x2 = *(const bf16x8*)(xb);)
    SB();
    acc1 = MFMA(U.w0, U.x3, acc1);
    LD(L.x3 = *(const bf16x8*)(xb + 256); L.w1 = tr_frag(wp + 8192, 1024);)
    SB();
    acc0 = MFMA(U.w1, U.x1, acc0);
    LD(L.x1 = *(const bf16x8*)(xa + 256); L.w2 = tr_frag(wp + 16384, 1024);)
    SB();
    acc1 = MFMA(U.w1, U.x2, acc1);
    LD(L.x0 = *(const bf16x8*)(xa);)
    SB();
    acc0 = MFMA(U.w2, U.x0, acc0);
    SB();
    acc1 = MFMA(U.w2, U.x1, acc1);
    SB();
  };
  Frags A, B;
  {
    const bf16x8 zero = {0};
    B.x0 = B.x1 = B.x2 = B.x3 = B.w0 = B.w1 = B.w2 = zero;
  }
  int wsl = 0, xsl = 0;
#pragma unroll 1
  for (int s = 0; s < NS; s += 2) {
    const char* xbase = smem + xsl * XSLOT;
#pragma unroll
    for (int o = 0; o < 2; ++o) {
      BARRIER();
      STAMP(1 + s + o);
      const char* wp = smem + wsl * WSLOT + wrd;
      half(A, B, wp, xbase + (xrd0 ^ (unsigned)(o * 64)), xbase + (xrd1 ^ (unsigned)(o * 64)));
      half(B, A, wp + 16 * 256, xbase + (xrd0 ^ (unsigned)(o * 64 + 32)), xbase + (xrd1 ^ (unsigned)(o * 64 + 32)));
      wsl = wsl + 1 == RING ? 0 : wsl + 1;
    }
    xsl = xsl + 1 == XRING ? 0 : xsl + 1;
  }
  acc0 = MFMA(B.w0, B.x2, acc0);
  acc1 = MFMA(B.w0, B.x3, acc1);
  acc0 = MFMA(B.w1, B.x1, acc0);
  acc1 = MFMA(B.w1, B.x2, acc1);
  acc0 = MFMA(B.w2, B.x0, acc0);
  acc1 = MFMA(B.w2, B.x1, acc1);
  STAMP(17); CSTAMP(21);

  const int kg = ni * 4 + wv;
#pragma unroll
  for (int m = 0; m < 2; ++m) {
    const f32x16& a = m ? acc1 : acc0;
    u32x4 o;
#pragma unroll
    for (int r = 0; r < 4; ++r) o[r] = pack_bf16(gate(a[2 * r], a[2 * r + 8]), gate(a[2 * r + 1], a[2 * r + 9]));
    zws[((mi * 2 + m) * 32 + kg) * 64 + lane] = o;
  }
  STAMP(18); CSTAMP(23);
  STAMP_DUMP(0, zws);
}

__global__ __launch_bounds__(256, 1) void wn_gemm2(const u32x4* __restrict__ zws, const uint16_t* __restrict__ wo,
                                                   uint16_t* __restrict__ out) {
  extern __shared__ __attribute__((aligned(256))) char smem[];
  constexpr int PROFOFF = LDS2; (void)PROFOFF;
  STAMP_INIT(); STAMP(0);
  const int tid = threadIdx.x, lane = tid & 63;
  const int wv = __builtin_amdgcn_readfirstlane(tid >> 6);
  const int bid = blockIdx.x, xcd = bid & 7, j = bid >> 3;
  const int mi = xcd * 4 + (j & 3), cj = j >> 2;
  const int b = mi >> 4, t0 = (mi & 15) * 64;
  const int tf = wv & 1, ch = wv >> 1;
  const i32x4 srd = make_srd(wo, DM * DM * 2);
  const unsigned sbase = (unsigned)(uintptr_t)(__attribute__((address_space(3))) char*)smem;
  const unsigned voff = (unsigned)((lane >> 3) * 1024 + cj * 128 + (((lane & 7) ^ (4 * ((lane >> 4) & 1))) * 16));
#pragma unroll
  for (int i = 0; i < 16; ++i) {
    const int k0 = (i * 4 + wv) * 8;
    dma16(srd, voff, (unsigned)(k0 * 1024), sbase + k0 * 128);
  }
  bf16x8 z[32];
  const u32x4* zp = zws + ((mi * 2 + tf) * 32) * 64 + lane;
#pragma unroll
  for (int k = 0; k < 32; ++k) z[k] = __builtin_bit_cast(bf16x8, zp[k * 64]);
  waitv<0>();
  BARRIER();
  STAMP(1);
  const int G = lane >> 4, q4 = (lane >> 2) & 3, p4 = lane & 3;
  const int cq = 4 * (p4 & 1) + 2 * (G & 1) + (p4 >> 1);
  const unsigned ard = (unsigned)((8 * (G >> 1) + q4) * 128 + ((ch ^ ((q4 >> 1) & 1)) * 64) + cq * 8);
  f32x16 acc = {0};
#pragma unroll
  for (int k = 0; k < 32; ++k) acc = MFMA(tr_frag(smem + ard + k * 16 * 128, 512), z[k], acc);
  const int c32 = lane & 31, h = lane >> 5;
  const int t = t0 + 4 * (c32 >> 1) + (c32 & 1) + 2 * tf;
  if (t < T_LEN) {
    u32x4 o0, o1;
#pragma unroll
    for (int r = 0; r < 4; ++r) {
      o0[r] = pack_bf16(acc[2 * r], acc[2 * r + 1]);
      o1[r] = pack_bf16(acc[8 + 2 * r], acc[8 + 2 * r + 1]);
    }
    u32x4* op = (u32x4*)(out + (size_t)(b * T_LEN + t) * DM + cj * 64 + ch * 32 + h * 16);
    op[0] = o0;
    op[1] = o1;
  }
  STAMP(2);
  STAMP_DUMP(1, zws);
}

extern "C" void kernel_launch(void* const* d_in, const int* in_sizes, int n_in, void* d_out, int out_size,
                              void* d_ws, size_t ws_size, hipStream_t stream) {
  const uint16_t* x = (const uint16_t*)d_in[0];
  const uint16_t* w = (const uint16_t*)d_in[1];
  const uint16_t* wo = (const uint16_t*)d_in[2];
  (void)hipFuncSetAttribute((const void*)wn_gemm1, hipFuncAttributeMaxDynamicSharedMemorySize, LDS1 + PROF_LDS);
  (void)hipFuncSetAttribute((const void*)wn_gemm2, hipFuncAttributeMaxDynamicSharedMemorySize, LDS2 + PROF_LDS);
  wn_gemm1<<<256, 512, LDS1 + PROF_LDS, stream>>>(x, w, (u32x4*)d_ws);
  wn_gemm2<<<256, 256, LDS2 + PROF_LDS, stream>>>((const u32x4*)d_ws, wo, (uint16_t*)d_out);
}
```

```cpp
#include <hip/hip_runtime.h>
#include <stdint.h>
#include <type_traits>

typedef short s16x4 __attribute__((ext_vector_type(4)));
typedef short s16x8 __attribute__((ext_vector_type(8)));
typedef __bf16 bf16x8 __attribute__((ext_vector_type(8)));
typedef __bf16 bf16x2 __attribute__((ext_vector_type(2)));
typedef float f32x16 __attribute__((ext_vector_type(16)));
typedef float f32x4 __attribute__((ext_vector_type(4)));
typedef float f32x2 __attribute__((ext_vector_type(2)));
typedef int i32x4 __attribute__((ext_vector_type(4)));
typedef unsigned u32x4 __attribute__((ext_vector_type(4)));
typedef unsigned u32x2 __attribute__((ext_vector_type(2)));
typedef __attribute__((address_space(3))) s16x4* lds_s16x4;

#define RING 5
#define GM 16
#define ROT 1
#define MAP2 1
#define PROF_LDS 0
#define STAMP(i)
#define CSTAMP(i)
#define STAMP_INIT()
#define STAMP_DUMP(k,base)
#define W0(x)

constexpr int T_LEN = 1000, DM = 512;
constexpr int WSLOT = 24576;
constexpr int XSLOT = 68 * 128;
constexpr int XRING = (RING + 2) / 2;
constexpr int XOFF = 0;
constexpr int WOFF = ((XRING * XSLOT + 255) / 256) * 256;
constexpr int LDS1 = WOFF + RING * WSLOT;
constexpr int WO_OFF = WOFF + WSLOT;
constexpr int NS = 16;
constexpr unsigned ZBYTES = 32u * 64u * 512u * 2u;
constexpr unsigned FLAG_OFF = ZBYTES;
constexpr unsigned EPOCH_OFF = ZBYTES + 256u * 128u;
constexpr int SPIN_MAX = 1 << 18;

__device__ __forceinline__ i32x4 make_srd(const void* p, unsigned bytes) {
  uint64_t a = (uint64_t)p;
  i32x4 r;
  r[0] = (int)(unsigned)a;
  r[1] = (int)(unsigned)(a >> 32);
  r[2] = (int)bytes;
  r[3] = 0x00020000;
  return r;
}

__device__ __forceinline__ void dma16(i32x4 srd, unsigned voff, unsigned soff, unsigned ldsaddr) {
  asm volatile("s_mov_b32 m0, %3\n\ts_nop 0\n\tbuffer_load_dwordx4 %0, %1, %2 offen lds"
               :: "v"(voff), "s"(srd), "s"(soff), "s"(ldsaddr));
}
__device__ __forceinline__ void dma16_lo8(i32x4 srd, unsigned voff, unsigned soff, unsigned ldsaddr) {
  asm volatile("s_mov_b32 m0, %3\n\ts_mov_b64 exec, 0xff\n\tbuffer_load_dwordx4 %0, %1, %2 offen lds\n\ts_mov_b64 exec, -1"
               :: "v"(voff), "s"(srd), "s"(soff), "s"(ldsaddr));
}
template <int N> __device__ __forceinline__ void waitv() { asm volatile("s_waitcnt vmcnt(%0)" :: "i"(N) : "memory"); }
#define BARRIER() asm volatile("s_waitcnt lgkmcnt(0)\n\ts_barrier" ::: "memory")
#define SB() __builtin_amdgcn_sched_barrier(0)

constexpr int stage_ops(int u) { return u < NS ? ((u & 1) ? 6 : 9) : (u < NS + 2 ? 6 : (u == NS + 2 ? 4 : 0)); }
constexpr int later(int s) {
  int n = 0;
  for (int d = 1; d <= RING - 2; ++d) n += stage_ops(s + d);
  return n;
}

__device__ __forceinline__ unsigned pack_bf16(float a, float b) {
  f32x2 v = {a, b};
  bf16x2 r = __builtin_convertvector(v, bf16x2);
  return __builtin_bit_cast(unsigned, r);
}

__device__ __forceinline__ float gate(float f, float g) {
  const float L2E = 1.4426950408889634f;
  float u = __builtin_amdgcn_exp2f(fminf(f * (-2.0f * L2E), 60.0f));
  float b = __builtin_amdgcn_exp2f(g * (-L2E));
  float t1 = 1.0f + u;
  float den = __builtin_fmaf(t1, b, t1);
  return (2.0f - t1) * __builtin_amdgcn_rcpf(den);
}

__device__ __forceinline__ bf16x8 tr_frag(const char* p, int step) {
  s16x4 lo = __builtin_amdgcn_ds_read_tr16_b64_v4i16((lds_s16x4)(p));
  s16x4 hi = __builtin_amdgcn_ds_read_tr16_b64_v4i16((lds_s16x4)(p + step));
  s16x8 v = __builtin_shufflevector(lo, hi, 0, 1, 2, 3, 4, 5, 6, 7);
  return __builtin_bit_cast(bf16x8, v);
}

struct Frags { bf16x8 x0, x1, x2, x3, w0, w1, w2; };
#define MFMA(a, b, c) __builtin_amdgcn_mfma_f32_32x32x16_bf16(a, b, c, 0, 0, 0)
#define MFMA16(a, b, c) __builtin_amdgcn_mfma_f32_16x16x32_bf16(a, b, c, 0, 0, 0)

template <int S, int E, class F> __device__ __forceinline__ void static_for(F&& f) {
  if constexpr (S < E) { f(std::integral_constant<int, S>{}); static_for<S + 1, E>(f); }
}

__device__ __forceinline__ u32x2 load8_dev(const char* p) {
  u32x2 v;
  asm volatile("global_load_dwordx2 %0, %1, off sc1\n\ts_waitcnt vmcnt(0)" : "=v"(v) : "v"(p) : "memory");
  return v;
}
__device__ __forceinline__ void store8_dev(char* p, u32x2 v) {
  asm volatile("global_store_dwordx2 %0, %1, off sc1" :: "v"(p), "v"(v) : "memory");
}

__global__ __launch_bounds__(512, 2) void wn_fused(const uint16_t* __restrict__ x, const uint16_t* __restrict__ w,
                                                   const uint16_t* __restrict__ wo, uint16_t* __restrict__ out,
                                                   char* __restrict__ ws) {
  extern __shared__ __attribute__((aligned(256))) char smem[];
  constexpr int PROFOFF = LDS1; (void)PROFOFF;
  const int tid = threadIdx.x, lane = tid & 63;
  const int wv8 = __builtin_amdgcn_readfirstlane(tid >> 6);
  STAMP_INIT(); W0(STAMP(0); CSTAMP(22));
  const int wv = wv8 & 3;
  const int bid = blockIdx.x, xcd = bid & 7, j = bid >> 3;
  constexpr int GN = 32 / GM, NGM = 32 / GM;
  const int mi = (xcd % NGM) * GM + (j % GM);
  const int ni = (xcd / NGM) * GN + (j / GM);
  const int b = mi >> 4, t0 = (mi & 15) * 64;
  const int mi2 = MAP2 ? xcd * 4 + (j & 3) : mi;
  const int cj = MAP2 ? (j >> 2) : ni;

  if (wv8 >= 4) {
    const int rot = ROT ? (((j % GM) * (GM >= 8 ? 1 : 8 / GM) + (j / GM) * (8 / (GN > 8 ? 8 : GN))) & 7) : 0;
    const i32x4 srdx = make_srd(x, 2 * T_LEN * DM * 2);
    const i32x4 srdw = make_srd(w, 3 * DM * 2 * DM * 2);
    const unsigned sbase = (unsigned)(uintptr_t)(__attribute__((address_space(3))) char*)smem;
    const unsigned wvoff = (unsigned)((lane >> 4) * 2048 + ((lane >> 3) & 1) * 1024 + ni * 128 + (((lane & 7) ^ (2 * (lane >> 4))) * 16));
    unsigned xvoff[3];
#pragma unroll
    for (int i = 0; i < 3; ++i) {
      int rho = i < 2 ? 16 * wv + 8 * i + (lane >> 3) : 64 + wv;
      int t = t0 - 4 + rho;
      int tc = t > T_LEN - 1 ? T_LEN - 1 : t;
      unsigned off = (unsigned)((b * T_LEN + tc) * 1024 + (((lane & 7) ^ ((rho >> 2) & 7)) * 16));
      xvoff[i] = t < 0 ? 0x80000000u : off;
    }
    auto issue = [&](int s, bool even, int wslot, int xslot) {
      const int cc = ((s >> 1) + rot) & 7;
      const int c0 = cc * 64 + (even ? 0 : 32);
      const unsigned wl = sbase + WOFF + wslot * WSLOT + 8 * wv * 256;
      const unsigned so = (unsigned)(c0 + 8 * wv) * 2048;
      if (even) {
        const unsigned xl = sbase + XOFF + xslot * XSLOT;
        dma16(srdx, xvoff[0], cc * 128, xl + (16 * wv) * 128);
        dma16(srdx, xvoff[1], cc * 128, xl + (16 * wv + 8) * 128);
        dma16_lo8(srdx, xvoff[2], cc * 128, xl + (64 + wv) * 128);
      }
#pragma unroll
      for (int tp = 0; tp < 3; ++tp)
#pragma unroll
        for (int i = 0; i < 2; ++i)
          dma16(srdw, wvoff, so + (unsigned)((tp * 512 + 4 * i) * 2048), wl + tp * 8192 + 4 * i * 256);
    };
    u32x2 ep;
    asm volatile("global_load_dwordx2 %0, %1, off sc1" : "=v"(ep) : "v"(ws + EPOCH_OFF) : "memory");
    static_for<0, RING - 1>([&](auto S) { constexpr int s = S; issue(s, (s & 1) == 0, s % RING, (s >> 1) % XRING); });
    constexpr int MAIN = ((NS - RING + 1) / 2) * 2;
    int wsl = (RING - 1) % RING, xs = (((RING - 1) + 1) >> 1) % XRING;
    const i32x4 srdo = make_srd(wo, DM * DM * 2);
    const unsigned ovoff = (unsigned)((lane >> 3) * 1024 + cj * 128 +
                                      (((lane & 7) ^ (2 * (((lane >> 4) & 1) | ((wv & 1) << 1)))) * 16));
#pragma unroll 1
    for (int s = 0; s < MAIN; s += 2) {
      waitv<later(0)>();
      BARRIER();
      {
        constexpr bool ev = ((RING - 1) & 1) == 0;
        issue(s + RING - 1, ev, wsl, xs);
        wsl = wsl + 1 == RING ? 0 : wsl + 1;
        if (ev) xs = xs + 1 == XRING ? 0 : xs + 1;
      }
      waitv<later(1)>();
      BARRIER();
      {
        constexpr bool ev = (RING & 1) == 0;
        issue(s + RING, ev, wsl, xs);
        wsl = wsl + 1 == RING ? 0 : wsl + 1;
        if (ev) xs = xs + 1 == XRING ? 0 : xs + 1;
      }
    }
    static_for<MAIN, NS>([&](auto S) {
      constexpr int s = S;
      waitv<later(s)>();
      BARRIER();
      constexpr int p = s + RING - 1 - NS;
      if constexpr (p >= 0 && p < 3) {
#pragma unroll
        for (int i = 6 * p; i < (p == 2 ? 16 : 6 * p + 6); ++i) {
          const int blk = 4 * i + wv;
          dma16(srdo, ovoff, (unsigned)(blk * 8192), sbase + WO_OFF + blk * 1024);
        }
      }
    });
    waitv<0>();
    asm volatile("" : "+v"(ep));
    BARRIER();
    u32x2 tag = {ep[0] + 1u, ep[1]};
    if (wv8 == 4) {
      if (lane == 0) store8_dev(ws + FLAG_OFF + (mi * 8 + ni) * 128, tag);
      const char* fp = ws + FLAG_OFF + (mi2 * 8 + (lane & 7)) * 128;
      for (int it = 0; it < SPIN_MAX; ++it) {
        u32x2 v = load8_dev(fp);
        if (__builtin_amdgcn_ballot_w64(v[0] != tag[0] || v[1] != tag[1]) == 0) break;
        __builtin_amdgcn_s_sleep(1);
      }
      STAMP(20);
      BARRIER();
    } else {
      BARRIER();
      if (wv8 == 5 && bid == 0) {
        for (int it = 0; it < SPIN_MAX; ++it) {
          bool bad = false;
#pragma unroll
          for (int i = 0; i < 4; ++i) {
            u32x2 v = load8_dev(ws + FLAG_OFF + (i * 64 + lane) * 128);
            bad = bad || v[0] != tag[0] || v[1] != tag[1];
          }
          if (__builtin_amdgcn_ballot_w64(bad) == 0) break;
          __builtin_amdgcn_s_sleep(1);
        }
        if (lane == 0) store8_dev(ws + EPOCH_OFF, tag);
      }
    }
    return;
  }

  const int G = lane >> 4, q4 = (lane >> 2) & 3, p4 = lane & 3;
  const int pperm = ((p4 & 1) << 1) | (p4 >> 1);
  const unsigned wrd = (unsigned)(WOFF + (8 * (G >> 1) + q4) * 256 + (G & 1) * 128 + ((wv ^ q4) * 32) + pperm * 8);
  const int c32 = lane & 31, h = lane >> 5, xq = c32 >> 1, xe = c32 & 1;
  const unsigned xrd0 = (unsigned)(XOFF + (4 * xq + xe) * 128 + ((h ^ (xq & 7)) * 16));
  const unsigned xrd1 = (unsigned)(XOFF + (4 * (xq + 1) + xe) * 128 + ((h ^ ((xq + 1) & 7)) * 16));

  f32x16 acc0 = {0}, acc1 = {0};
  auto half = [&](Frags& L, const Frags& U, const char* wp, const char* xa, const char* xb) {
    acc0 = MFMA(U.w0, U.x2, acc0);
    L.w0 = tr_frag(wp, 1024); L.x2 = *(const bf16x8*)(xb);
    SB();
    acc1 = MFMA(U.w0, U.x3, acc1);
    L.x3 = *(const bf16x8*)(xb + 256); L.w1 = tr_frag(wp + 8192, 1024);
    SB();
    acc0 = MFMA(U.w1, U.x1, acc0);
    L.x1 = *(const bf16x8*)(xa + 256); L.w2 = tr_frag(wp + 16384, 1024);
    SB();
    acc1 = MFMA(U.w1, U.x2, acc1);
    L.x0 = *(const bf16x8*)(xa);
    SB();
    acc0 = MFMA(U.w2, U.x0, acc0);
    SB();
    acc1 = MFMA(U.w2, U.x1, acc1);
    SB();
  };
  Frags A, B;
  {
    const bf16x8 zero = {0};
    B.x0 = B.x1 = B.x2 = B.x3 = B.w0 = B.w1 = B.w2 = zero;
  }
  int wsl = 0, xsl = 0;
#pragma unroll 1
  for (int s = 0; s < NS; s += 2) {
    const char* xbase = smem + xsl * XSLOT;
#pragma unroll
    for (int o = 0; o < 2; ++o) {
      BARRIER();
      const char* wp = smem + wsl * WSLOT + wrd;
      half(A, B, wp, xbase + (xrd0 ^ (unsigned)(o * 64)), xbase + (xrd1 ^ (unsigned)(o * 64)));
      half(B, A, wp + 16 * 256, xbase + (xrd0 ^ (unsigned)(o * 64 + 32)), xbase + (xrd1 ^ (unsigned)(o * 64 + 32)));
      wsl = wsl + 1 == RING ? 0 : wsl + 1;
    }
    xsl = xsl + 1 == XRING ? 0 : xsl + 1;
  }
  acc0 = MFMA(B.w0, B.x2, acc0);
  acc1 = MFMA(B.w0, B.x3, acc1);
  acc0 = MFMA(B.w1, B.x1, acc0);
  acc1 = MFMA(B.w1, B.x2, acc1);
  acc0 = MFMA(B.w2, B.x0, acc0);
  acc1 = MFMA(B.w2, B.x1, acc1);
  W0(STAMP(3));

  const __amdgpu_buffer_rsrc_t zr = __builtin_amdgcn_make_buffer_rsrc(ws, 0, ZBYTES, 0x00020000);
  {
    const int kg = ni * 4 + wv;
    const unsigned zo = (unsigned)((((mi * 4 + (c32 >> 4)) * 16 + (kg >> 1)) * 64 + ((kg & 1) * 2 + h) * 16 + (c32 & 15)) * 16);
#pragma unroll
    for (int m = 0; m < 2; ++m) {
      const f32x16& a = m ? acc1 : acc0;
      u32x4 o;
#pragma unroll
      for (int r = 0; r < 4; ++r) o[r] = pack_bf16(gate(a[2 * r], a[2 * r + 8]), gate(a[2 * r + 1], a[2 * r + 9]));
      __builtin_amdgcn_raw_buffer_store_b128(o, zr, zo + m * 2 * 16 * 1024, 0, 16);
    }
  }
  W0(STAMP(4));
  waitv<0>();
  W0(STAMP(5));
  BARRIER();
  W0(STAMP(6));

  const int f2 = ((q4 >> 1) & 1) | ((G & 1) << 1);
  const unsigned ard = (unsigned)(WO_OFF + (8 * G + q4) * 128 + f2 * 32 + p4 * 8);
  bf16x8 wa[8][4];
#pragma unroll
  for (int ks = 0; ks < 8; ++ks)
#pragma unroll
    for (int ct = 0; ct < 4; ++ct) wa[ks][ct] = tr_frag(smem + (ard ^ (unsigned)(ct * 32)) + ks * 4096, 512);
  BARRIER();
  W0(STAMP(7));
  u32x4 z[16];
  {
    const unsigned zo = (unsigned)((((mi2 * 4 + wv) * 16) * 64 + lane) * 16);
#pragma unroll
    for (int ks = 0; ks < 16; ++ks) z[ks] = __builtin_amdgcn_raw_buffer_load_b128(zr, zo, ks * 1024, 16);
  }
  SB();
  f32x4 acc[4] = {{0}, {0}, {0}, {0}};
#pragma unroll
  for (int ks = 0; ks < 8; ++ks) {
#pragma unroll
    for (int ct = 0; ct < 4; ++ct) acc[ct] = MFMA16(wa[ks][ct], __builtin_bit_cast(bf16x8, z[ks]), acc[ct]);
#pragma unroll
    for (int ct = 0; ct < 4; ++ct) wa[ks][ct] = tr_frag(smem + (ard ^ (unsigned)(ct * 32)) + (ks + 8) * 4096, 512);
    SB();
  }
  W0(STAMP(8));
#pragma unroll
  for (int ks = 0; ks < 8; ++ks) {
#pragma unroll
    for (int ct = 0; ct < 4; ++ct) acc[ct] = MFMA16(wa[ks][ct], __builtin_bit_cast(bf16x8, z[ks + 8]), acc[ct]);
    SB();
  }
  W0(STAMP(9));
  {
    const int cc = 16 * (wv & 1) + (lane & 15);
    const int t = (mi2 & 15) * 64 + 4 * (cc >> 1) + (cc & 1) + 2 * (wv >> 1);
    if (t < T_LEN) {
      uint16_t* op = out + (size_t)((mi2 >> 4) * T_LEN + t) * DM + cj * 64 + 4 * G;
#pragma unroll
      for (int ct = 0; ct < 4; ++ct) {
        u32x2 o = {pack_bf16(acc[ct][0], acc[ct][1]), pack_bf16(acc[ct][2], acc[ct][3])};
        *(u32x2*)(op + ct * 16) = o;
      }
    }
  }
  W0(STAMP(18); CSTAMP(23));
}

extern "C" void kernel_launch(void* const* d_in, const int* in_sizes, int n_in, void* d_out, int out_size,
                              void* d_ws, size_t ws_size, hipStream_t stream) {
  const uint16_t* x = (const uint16_t*)d_in[0];
  const uint16_t* w = (const uint16_t*)d_in[1];
  const uint16_t* wo = (const uint16_t*)d_in[2];
  (void)hipFuncSetAttribute((const void*)wn_fused, hipFuncAttributeMaxDynamicSharedMemorySize, LDS1 + PROF_LDS);
  wn_fused<<<256, 512, LDS1 + PROF_LDS, stream>>>(x, w, wo, (uint16_t*)d_out, (char*)d_ws);
}
```

```cpp
#include <hip/hip_runtime.h>
#include <stdint.h>
#include <type_traits>

typedef short s16x4 __attribute__((ext_vector_type(4)));
typedef short s16x8 __attribute__((ext_vector_type(8)));
typedef __bf16 bf16x8 __attribute__((ext_vector_type(8)));
typedef __bf16 bf16x2 __attribute__((ext_vector_type(2)));
typedef float f32x16 __attribute__((ext_vector_type(16)));
typedef float f32x4 __attribute__((ext_vector_type(4)));
typedef float f32x2 __attribute__((ext_vector_type(2)));
typedef int i32x4 __attribute__((ext_vector_type(4)));
typedef unsigned u32x4 __attribute__((ext_vector_type(4)));
typedef unsigned u32x2 __attribute__((ext_vector_type(2)));
typedef __attribute__((address_space(3))) s16x4* lds_s16x4;

#define RING 5
#define GM 16
#define ROT 1
#define CODEPF 1
#define CODE_BYTES 10240
#define MAP2 1
#define PROF_LDS 0
#define STAMP(i)
#define CSTAMP(i)
#define STAMP_INIT()
#define STAMP_DUMP(k,base)
#define W0(x)

constexpr int T_LEN = 1000, DM = 512;
constexpr int WSLOT = 24576;
constexpr int XSLOT = 68 * 128;
constexpr int XRING = (RING + 2) / 2;
constexpr int XOFF = 0;
constexpr int WOFF = ((XRING * XSLOT + 255) / 256) * 256;
constexpr int LDS1 = WOFF + RING * WSLOT;
constexpr int WO_OFF = WOFF + WSLOT;
constexpr int NS = 16;
constexpr unsigned ZBYTES = 32u * 64u * 512u * 2u;
constexpr unsigned FLAG_OFF = ZBYTES;
constexpr unsigned EPOCH_OFF = ZBYTES + 256u * 128u;
constexpr int SPIN_MAX = 1 << 18;

__device__ __forceinline__ i32x4 make_srd(const void* p, unsigned bytes) {
  uint64_t a = (uint64_t)p;
  i32x4 r;
  r[0] = (int)(unsigned)a;
  r[1] = (int)(unsigned)(a >> 32);
  r[2] = (int)bytes;
  r[3] = 0x00020000;
  return r;
}

__device__ __forceinline__ void dma16(i32x4 srd, unsigned voff, unsigned soff, unsigned ldsaddr) {
  asm volatile("s_mov_b32 m0, %3\n\ts_nop 0\n\tbuffer_load_dwordx4 %0, %1, %2 offen lds"
               :: "v"(voff), "s"(srd), "s"(soff), "s"(ldsaddr));
}
__device__ __forceinline__ void dma16_lo8(i32x4 srd, unsigned voff, unsigned soff, unsigned ldsaddr) {
  asm volatile("s_mov_b32 m0, %3\n\ts_mov_b64 exec, 0xff\n\tbuffer_load_dwordx4 %0, %1, %2 offen lds\n\ts_mov_b64 exec, -1"
               :: "v"(voff), "s"(srd), "s"(soff), "s"(ldsaddr));
}
template <int N> __device__ __forceinline__ void waitv() { asm volatile("s_waitcnt vmcnt(%0)" :: "i"(N) : "memory"); }
#define BARRIER() asm volatile("s_waitcnt lgkmcnt(0)\n\ts_barrier" ::: "memory")
#define SB() __builtin_amdgcn_sched_barrier(0)

constexpr int stage_ops(int u) { return u < NS ? ((u & 1) ? 6 : 9) : (u == NS ? 7 : (u == NS + 1 ? 6 : (u == NS + 2 ? 4 : 0))); }
constexpr int later(int s) {
  int n = 0;
  for (int d = 1; d <= RING - 2; ++d) n += stage_ops(s + d);
  return n;
}

__device__ __forceinline__ unsigned pack_bf16(float a, float b) {
  f32x2 v = {a, b};
  bf16x2 r = __builtin_convertvector(v, bf16x2);
  return __builtin_bit_cast(unsigned, r);
}

__device__ __forceinline__ float gate(float f, float g) {
  const float L2E = 1.4426950408889634f;
  float u = __builtin_amdgcn_exp2f(fminf(f * (-2.0f * L2E), 60.0f));
  float b = __builtin_amdgcn_exp2f(g * (-L2E));
  float t1 = 1.0f + u;
  float den = __builtin_fmaf(t1, b, t1);
  return (2.0f - t1) * __builtin_amdgcn_rcpf(den);
}

__device__ __forceinline__ bf16x8 tr_frag(const char* p, int step) {
  s16x4 lo = __builtin_amdgcn_ds_read_tr16_b64_v4i16((lds_s16x4)(p));
  s16x4 hi = __builtin_amdgcn_ds_read_tr16_b64_v4i16((lds_s16x4)(p + step));
  s16x8 v = __builtin_shufflevector(lo, hi, 0, 1, 2, 3, 4, 5, 6, 7);
  return __builtin_bit_cast(bf16x8, v);
}

struct Frags { bf16x8 x0, x1, x2, x3, w0, w1, w2; };
#define MFMA(a, b, c) __builtin_amdgcn_mfma_f32_32x32x16_bf16(a, b, c, 0, 0, 0)
#define MFMA16(a, b, c) __builtin_amdgcn_mfma_f32_16x16x32_bf16(a, b, c, 0, 0, 0)

template <int S, int E, class F> __device__ __forceinline__ void static_for(F&& f) {
  if constexpr (S < E) { f(std::integral_constant<int, S>{}); static_for<S + 1, E>(f); }
}

__device__ __forceinline__ u32x2 load8_dev(const char* p) {
  u32x2 v;
  asm volatile("global_load_dwordx2 %0, %1, off sc1\n\ts_waitcnt vmcnt(0)" : "=v"(v) : "v"(p) : "memory");
  return v;
}
__device__ __forceinline__ void store8_dev(char* p, u32x2 v) {
  asm volatile("global_store_dwordx2 %0, %1, off sc1" :: "v"(p), "v"(v) : "memory");
}

__global__ __launch_bounds__(512, 2) void wn_fused(const uint16_t* __restrict__ x, const uint16_t* __restrict__ w,
                                                   const uint16_t* __restrict__ wo, uint16_t* __restrict__ out,
                                                   char* __restrict__ ws) {
  extern __shared__ __attribute__((aligned(256))) char smem[];
  constexpr int PROFOFF = LDS1 + 256; (void)PROFOFF;
  const int tid = threadIdx.x, lane = tid & 63;
  const int wv8 = __builtin_amdgcn_readfirstlane(tid >> 6);
  {
    uint64_t pc;
    asm volatile("s_getpc_b64 %0" : "=s"(pc));
    if (wv8 < 2) {
      const i32x4 srdc = make_srd((const void*)(pc & ~(uint64_t)255), CODE_BYTES);
      const unsigned sb = (unsigned)(uintptr_t)(__attribute__((address_space(3))) char*)smem;
      asm volatile("s_mov_b32 m0, %2\n\ts_nop 0\n\tbuffer_load_dword %0, %1, 0 offen lds"
                   :: "v"((unsigned)tid * 128u), "s"(srdc), "s"(sb + LDS1));
    }
  }
  STAMP_INIT(); W0(STAMP(0); CSTAMP(22));
  const int wv = wv8 & 3;
  const int bid = blockIdx.x, xcd = bid & 7, j = bid >> 3;
  constexpr int GN = 32 / GM, NGM = 32 / GM;
  const int mi = (xcd % NGM) * GM + (j % GM);
  const int ni = (xcd / NGM) * GN + (j / GM);
  const int b = mi >> 4, t0 = (mi & 15) * 64;
  const int mi2 = MAP2 ? xcd * 4 + (j & 3) : mi;
  const int cj = MAP2 ? (j >> 2) : ni;

  if (__builtin_expect(wv8 >= 4, 1)) {
    const int rot = ROT ? (((j % GM) * (GM >= 8 ? 1 : 8 / GM) + (j / GM) * (8 / (GN > 8 ? 8 : GN))) & 7) : 0;
    const i32x4 srdx = make_srd(x, 2 * T_LEN * DM * 2);
    const i32x4 srdw = make_srd(w, 3 * DM * 2 * DM * 2);
    const unsigned sbase = (unsigned)(uintptr_t)(__attribute__((address_space(3))) char*)smem;
    const unsigned wvoff = (unsigned)((lane >> 4) * 2048 + ((lane >> 3) & 1) * 1024 + ni * 128 + (((lane & 7) ^ (2 * (lane >> 4))) * 16));
    unsigned xvoff[3];
#pragma unroll
    for (int i = 0; i < 3; ++i) {
      int rho = i < 2 ? 16 * wv + 8 * i + (lane >> 3) : 64 + wv;
      int t = t0 - 4 + rho;
      int tc = t > T_LEN - 1 ? T_LEN - 1 : t;
      unsigned off = (unsigned)((b * T_LEN + tc) * 1024 + (((lane & 7) ^ ((rho >> 2) & 7)) * 16));
      xvoff[i] = t < 0 ? 0x80000000u : off;
    }
    auto issue = [&](int s, bool even, int wslot, int xslot) {
      const int cc = ((s >> 1) + rot) & 7;
      const int c0 = cc * 64 + (even ? 0 : 32);
      const unsigned wl = sbase + WOFF + wslot * WSLOT + 8 * wv * 256;
      const unsigned so = (unsigned)(c0 + 8 * wv) * 2048;
#pragma unroll
      for (int tp = 0; tp < 3; ++tp)
#pragma unroll
        for (int i = 0; i < 2; ++i)
          dma16(srdw, wvoff, so + (unsigned)((tp * 512 + 4 * i) * 2048), wl + tp * 8192 + 4 * i * 256);
      if (even) {
        const unsigned xl = sbase + XOFF + xslot * XSLOT;
        dma16(srdx, xvoff[0], cc * 128, xl + (16 * wv) * 128);
        dma16(srdx, xvoff[1], cc * 128, xl + (16 * wv + 8) * 128);
        dma16_lo8(srdx, xvoff[2], cc * 128, xl + (64 + wv) * 128);
      }
    };
    u32x2 ep;
    static_for<0, RING - 1>([&](auto S) { constexpr int s = S; issue(s, (s & 1) == 0, s % RING, (s >> 1) % XRING);
    });
    constexpr int MAIN = ((NS - RING + 1) / 2) * 2;
    int wsl = (RING - 1) % RING, xs = (((RING - 1) + 1) >> 1) % XRING;
    const i32x4 srdo = make_srd(wo, DM * DM * 2);
    const unsigned ovoff = (unsigned)((lane >> 3) * 1024 + cj * 128 +
                                      (((lane & 7) ^ (2 * (((lane >> 4) & 1) | ((wv & 1) << 1)))) * 16));
#pragma unroll 1
    for (int s = 0; s < MAIN; s += 2) {
      waitv<later(0)>();
      BARRIER();
      {
        constexpr bool ev = ((RING - 1) & 1) == 0;
        issue(s + RING - 1, ev, wsl, xs);
        wsl = wsl + 1 == RING ? 0 : wsl + 1;
        if (ev) xs = xs + 1 == XRING ? 0 : xs + 1;
      }
      waitv<later(1)>();
      BARRIER();
      {
        constexpr bool ev = (RING & 1) == 0;
        issue(s + RING, ev, wsl, xs);
        wsl = wsl + 1 == RING ? 0 : wsl + 1;
        if (ev) xs = xs + 1 == XRING ? 0 : xs + 1;
      }
    }
    static_for<MAIN, NS>([&](auto S) {
      constexpr int s = S;
      waitv<later(s)>();
      BARRIER();
      constexpr int p = s + RING - 1 - NS;
      if constexpr (p == 0)
        asm volatile("global_load_dwordx2 %0, %1, off sc1" : "=v"(ep) : "v"(ws + EPOCH_OFF) : "memory");
      if constexpr (p >= 0 && p < 3) {
#pragma unroll
        for (int i = 6 * p; i < (p == 2 ? 16 : 6 * p + 6); ++i) {
          const int blk = 4 * i + wv;
          dma16(srdo, ovoff, (unsigned)(blk * 8192), sbase + WO_OFF + blk * 1024);
        }
      }
    });
    waitv<0>();
    asm volatile("" : "+v"(ep));
    BARRIER();
    u32x2 tag = {ep[0] + 1u, ep[1]};
    if (wv8 == 4) {
      if (lane == 0) store8_dev(ws + FLAG_OFF + (mi * 8 + ni) * 128, tag);
      const char* fp = ws + FLAG_OFF + (mi2 * 8 + (lane & 7)) * 128;
      for (int it = 0; it < SPIN_MAX; ++it) {
        u32x2 v = load8_dev(fp);
        if (__builtin_amdgcn_ballot_w64(v[0] != tag[0] || v[1] != tag[1]) == 0) break;
        __builtin_amdgcn_s_sleep(1);
      }
      STAMP(20);
      BARRIER();
    } else {
      BARRIER();
      if (wv8 == 5 && bid == 0) {
        for (int it = 0; it < SPIN_MAX; ++it) {
          bool bad = false;
#pragma unroll
          for (int i = 0; i < 4; ++i) {
            u32x2 v = load8_dev(ws + FLAG_OFF + (i * 64 + lane) * 128);
            bad = bad || v[0] != tag[0] || v[1] != tag[1];
          }
          if (__builtin_amdgcn_ballot_w64(bad) == 0) break;
          __builtin_amdgcn_s_sleep(1);
        }
        if (lane == 0) store8_dev(ws + EPOCH_OFF, tag);
      }
    }
    return;
  }

  const int G = lane >> 4, q4 = (lane >> 2) & 3, p4 = lane & 3;
  const int pperm = ((p4 & 1) << 1) | (p4 >> 1);
  const unsigned wrd = (unsigned)(WOFF + (8 * (G >> 1) + q4) * 256 + (G & 1) * 128 + ((wv ^ q4) * 32) + pperm * 8);
  const int c32 = lane & 31, h = lane >> 5, xq = c32 >> 1, xe = c32 & 1;
  const unsigned xrd0 = (unsigned)(XOFF + (4 * xq + xe) * 128 + ((h ^ (xq & 7)) * 16));
  const unsigned xrd1 = (unsigned)(XOFF + (4 * (xq + 1) + xe) * 128 + ((h ^ ((xq + 1) & 7)) * 16));

  f32x16 acc0 = {0}, acc1 = {0};
  auto half = [&](Frags& L, const Frags& U, const char* wp, const char* xa, const char* xb) {
    acc0 = MFMA(U.w0, U.x2, acc0);
    L.w0 = tr_frag(wp, 1024); L.x2 = *(const bf16x8*)(xb);
    SB();
    acc1 = MFMA(U.w0, U.x3, acc1);
    L.x3 = *(const bf16x8*)(xb + 256); L.w1 = tr_frag(wp + 8192, 1024);
    SB();
    acc0 = MFMA(U.w1, U.x1, acc0);
    L.x1 = *(const bf16x8*)(xa + 256); L.w2 = tr_frag(wp + 16384, 1024);
    SB();
    acc1 = MFMA(U.w1, U.x2, acc1);
    L.x0 = *(const bf16x8*)(xa);
    SB();
    acc0 = MFMA(U.w2, U.x0, acc0);
    SB();
    acc1 = MFMA(U.w2, U.x1, acc1);
    SB();
  };
  Frags A, B;
  {
    const bf16x8 zero = {0};
    B.x0 = B.x1 = B.x2 = B.x3 = B.w0 = B.w1 = B.w2 = zero;
  }
  int wsl = 0, xsl = 0;
#pragma unroll 1
  for (int s = 0; s < NS; s += 2) {
    const char* xbase = smem + xsl * XSLOT;
#pragma unroll
    for (int o = 0; o < 2; ++o) {
      BARRIER();
      const char* wp = smem + wsl * WSLOT + wrd;
      half(A, B, wp, xbase + (xrd0 ^ (unsigned)(o * 64)), xbase + (xrd1 ^ (unsigned)(o * 64)));
      half(B, A, wp + 16 * 256, xbase + (xrd0 ^ (unsigned)(o * 64 + 32)), xbase + (xrd1 ^ (unsigned)(o * 64 + 32)));
      wsl = wsl + 1 == RING ? 0 : wsl + 1;
    }
    xsl = xsl + 1 == XRING ? 0 : xsl + 1;
  }
  acc0 = MFMA(B.w0, B.x2, acc0);
  acc1 = MFMA(B.w0, B.x3, acc1);
  acc0 = MFMA(B.w1, B.x1, acc0);
  acc1 = MFMA(B.w1, B.x2, acc1);
  acc0 = MFMA(B.w2, B.x0, acc0);
  acc1 = MFMA(B.w2, B.x1, acc1);
  W0(STAMP(3));

  const __amdgpu_buffer_rsrc_t zr = __builtin_amdgcn_make_buffer_rsrc(ws, 0, ZBYTES, 0x00020000);
  {
    const int kg = ni * 4 + wv;
    const unsigned zo = (unsigned)((((mi * 4 + (c32 >> 4)) * 16 + (kg >> 1)) * 64 + ((kg & 1) * 2 + h) * 16 + (c32 & 15)) * 16);
#pragma unroll
    for (int m = 0; m < 2; ++m) {
      const f32x16& a = m ? acc1 : acc0;
      u32x4 o;
#pragma unroll
      for (int r = 0; r < 4; ++r) o[r] = pack_bf16(gate(a[2 * r], a[2 * r + 8]), gate(a[2 * r + 1], a[2 * r + 9]));
      __builtin_amdgcn_raw_buffer_store_b128(o, zr, zo + m * 2 * 16 * 1024, 0, 16);
    }
  }
  W0(STAMP(4));
  waitv<0>();
  W0(STAMP(5));
  BARRIER();
  W0(STAMP(6));

  const int f2 = ((q4 >> 1) & 1) | ((G & 1) << 1);
  const unsigned ard = (unsigned)(WO_OFF + (8 * G + q4) * 128 + f2 * 32 + p4 * 8);
  bf16x8 wa[8][4];
#pragma unroll
  for (int ks = 0; ks < 8; ++ks)
#pragma unroll
    for (int ct = 0; ct < 4; ++ct) wa[ks][ct] = tr_frag(smem + (ard ^ (unsigned)(ct * 32)) + ks * 4096, 512);
  BARRIER();
  W0(STAMP(7));
  u32x4 z[16];
  {
    const unsigned zo = (unsigned)((((mi2 * 4 + wv) * 16) * 64 + lane) * 16);
#pragma unroll
    for (int ks = 0; ks < 16; ++ks) z[ks] = __builtin_amdgcn_raw_buffer_load_b128(zr, zo, ks * 1024, 16);
  }
  SB();
  f32x4 acc[4] = {{0}, {0}, {0}, {0}};
#pragma unroll
  for (int ks = 0; ks < 8; ++ks) {
#pragma unroll
    for (int ct = 0; ct < 4; ++ct) acc[ct] = MFMA16(wa[ks][ct], __builtin_bit_cast(bf16x8, z[ks]), acc[ct]);
#pragma unroll
    for (int ct = 0; ct < 4; ++ct) wa[ks][ct] = tr_frag(smem + (ard ^ (unsigned)(ct * 32)) + (ks + 8) * 4096, 512);
    SB();
  }
  W0(STAMP(8));
#pragma unroll
  for (int ks = 0; ks < 8; ++ks) {
#pragma unroll
    for (int ct = 0; ct < 4; ++ct) acc[ct] = MFMA16(wa[ks][ct], __builtin_bit_cast(bf16x8, z[ks + 8]), acc[ct]);
    SB();
  }
  W0(STAMP(9));
  {
    const int cc = 16 * (wv & 1) + (lane & 15);
    const int t = (mi2 & 15) * 64 + 4 * (cc >> 1) + (cc & 1) + 2 * (wv >> 1);
    if (t < T_LEN) {
      uint16_t* op = out + (size_t)((mi2 >> 4) * T_LEN + t) * DM + cj * 64 + 4 * G;
#pragma unroll
      for (int ct = 0; ct < 4; ++ct) {
        u32x2 o = {pack_bf16(acc[ct][0], acc[ct][1]), pack_bf16(acc[ct][2], acc[ct][3])};
        *(u32x2*)(op + ct * 16) = o;
      }
    }
  }
  W0(STAMP(18); CSTAMP(23));
}

extern "C" void kernel_launch(void* const* d_in, const int* in_sizes, int n_in, void* d_out, int out_size,
                              void* d_ws, size_t ws_size, hipStream_t stream) {
  const uint16_t* x = (const uint16_t*)d_in[0];
  const uint16_t* w = (const uint16_t*)d_in[1];
  const uint16_t* wo = (const uint16_t*)d_in[2];
  (void)hipFuncSetAttribute((const void*)wn_fused, hipFuncAttributeMaxDynamicSharedMemorySize, LDS1 + 256 + PROF_LDS);
  wn_fused<<<256, 512, LDS1 + 256 + PROF_LDS, stream>>>(x, w, wo, (uint16_t*)d_out, (char*)d_ws);
}
```

```cpp
#include <hip/hip_runtime.h>
#include <stdint.h>
#include <type_traits>

typedef short s16x4 __attribute__((ext_vector_type(4)));
typedef short s16x8 __attribute__((ext_vector_type(8)));
typedef __bf16 bf16x8 __attribute__((ext_vector_type(8)));
typedef __bf16 bf16x2 __attribute__((ext_vector_type(2)));
typedef float f32x16 __attribute__((ext_vector_type(16)));
typedef float f32x4 __attribute__((ext_vector_type(4)));
typedef float f32x2 __attribute__((ext_vector_type(2)));
typedef int i32x4 __attribute__((ext_vector_type(4)));
typedef unsigned u32x4 __attribute__((ext_vector_type(4)));
typedef unsigned u32x2 __attribute__((ext_vector_type(2)));
typedef __attribute__((address_space(3))) s16x4* lds_s16x4;

#define RING 5
#define GM 16
#define ROT 1
#define CODEPF 1
#define CODE_BYTES 11776
#define MAP2 1
#define LAG 1
#define PROF_LDS 0
#define STAMP(i)
#define CSTAMP(i)
#define STAMP_INIT()
#define STAMP_DUMP(k,base)
#define W0(x)

constexpr int T_LEN = 1000, DM = 512;
constexpr int WSLOT = 24576;
constexpr int XSLOT = 68 * 128;
constexpr int XRING = (RING + 2) / 2;
constexpr int XOFF = 0;
constexpr int WOFF = ((XRING * XSLOT + 255) / 256) * 256;
constexpr int LDS1 = WOFF + RING * WSLOT;
constexpr int WO_OFF = WOFF + WSLOT;
constexpr int NS = 16;
constexpr unsigned ZBYTES = 32u * 64u * 512u * 2u;
constexpr unsigned FLAG_OFF = ZBYTES;
constexpr unsigned EPOCH_OFF = ZBYTES + 256u * 128u;
constexpr int SPIN_MAX = 1 << 18;

__device__ __forceinline__ i32x4 make_srd(const void* p, unsigned bytes) {
  uint64_t a = (uint64_t)p;
  i32x4 r;
  r[0] = (int)(unsigned)a;
  r[1] = (int)(unsigned)(a >> 32);
  r[2] = (int)bytes;
  r[3] = 0x00020000;
  return r;
}

__device__ __forceinline__ void dma16(i32x4 srd, unsigned voff, unsigned soff, unsigned ldsaddr) {
  asm volatile("s_mov_b32 m0, %3\n\ts_nop 0\n\tbuffer_load_dwordx4 %0, %1, %2 offen lds"
               :: "v"(voff), "s"(srd), "s"(soff), "s"(ldsaddr));
}
__device__ __forceinline__ void dma16_lo8(i32x4 srd, unsigned voff, unsigned soff, unsigned ldsaddr) {
  asm volatile("s_mov_b32 m0, %3\n\ts_mov_b64 exec, 0xff\n\tbuffer_load_dwordx4 %0, %1, %2 offen lds\n\ts_mov_b64 exec, -1"
               :: "v"(voff), "s"(srd), "s"(soff), "s"(ldsaddr));
}
template <int N> __device__ __forceinline__ void waitv() { asm volatile("s_waitcnt vmcnt(%0)" :: "i"(N) : "memory"); }
#define BARRIER() asm volatile("s_waitcnt lgkmcnt(0)\n\ts_barrier" ::: "memory")
#define SB() __builtin_amdgcn_sched_barrier(0)
#define CBARRIER() do { __builtin_amdgcn_s_waitcnt(0xC07F); asm volatile("s_barrier" ::: "memory"); SB(); } while (0)
#define MFMA16_ACC(acc, a, b) asm volatile("v_mfma_f32_16x16x32_bf16 %0, %1, %2, %0" : "+v"(acc) : "v"(a), "v"(b))

constexpr int stage_ops(int u) { return u < NS ? ((u & 1) ? 6 : 9) : (u == NS ? 7 : (u == NS + 1 ? 6 : (u == NS + 2 ? 4 : 0))); }
constexpr int later(int s) {
  int n = 0;
  for (int d = 1; d <= RING - 2; ++d) n += stage_ops(s + d);
  return n;
}

__device__ __forceinline__ unsigned pack_bf16(float a, float b) {
  f32x2 v = {a, b};
  bf16x2 r = __builtin_convertvector(v, bf16x2);
  return __builtin_bit_cast(unsigned, r);
}

__device__ __forceinline__ float gate(float f, float g) {
  const float L2E = 1.4426950408889634f;
  float u = __builtin_amdgcn_exp2f(fminf(f * (-2.0f * L2E), 60.0f));
  float b = __builtin_amdgcn_exp2f(g * (-L2E));
  float t1 = 1.0f + u;
  float den = __builtin_fmaf(t1, b, t1);
  return (2.0f - t1) * __builtin_amdgcn_rcpf(den);
}

__device__ __forceinline__ bf16x8 tr_frag(const char* p, int step) {
  s16x4 lo = __builtin_amdgcn_ds_read_tr16_b64_v4i16((lds_s16x4)(p));
  s16x4 hi = __builtin_amdgcn_ds_read_tr16_b64_v4i16((lds_s16x4)(p + step));
  s16x8 v = __builtin_shufflevector(lo, hi, 0, 1, 2, 3, 4, 5, 6, 7);
  return __builtin_bit_cast(bf16x8, v);
}

typedef __attribute__((address_space(3))) bf16x8* lds_bf16x8;
__device__ __forceinline__ bf16x8 tr_frag_l(unsigned a, int step) {
  s16x4 lo = __builtin_amdgcn_ds_read_tr16_b64_v4i16((lds_s16x4)(uintptr_t)(a));
  s16x4 hi = __builtin_amdgcn_ds_read_tr16_b64_v4i16((lds_s16x4)(uintptr_t)(a + step));
  s16x8 v = __builtin_shufflevector(lo, hi, 0, 1, 2, 3, 4, 5, 6, 7);
  return __builtin_bit_cast(bf16x8, v);
}
struct Frags { bf16x8 x[6], wf[3], wg[3]; };
__device__ __forceinline__ int xsw(int q) { return (int)((0x240674032ull >> (4 * q)) & 7); }
#define MFMA16(a, b, c) __builtin_amdgcn_mfma_f32_16x16x32_bf16(a, b, c, 0, 0, 0)

template <int S, int E, class F> __device__ __forceinline__ void static_for(F&& f) {
  if constexpr (S < E) { f(std::integral_constant<int, S>{}); static_for<S + 1, E>(f); }
}

__device__ __forceinline__ u32x2 load8_dev(const char* p) {
  u32x2 v;
  asm volatile("global_load_dwordx2 %0, %1, off sc1\n\ts_waitcnt vmcnt(0)" : "=v"(v) : "v"(p) : "memory");
  return v;
}
__device__ __forceinline__ void store8_dev(char* p, u32x2 v) {
  asm volatile("global_store_dwordx2 %0, %1, off sc1" :: "v"(p), "v"(v) : "memory");
}

__global__ __launch_bounds__(512, 2) void wn_fused(const uint16_t* __restrict__ x, const uint16_t* __restrict__ w,
                                                   const uint16_t* __restrict__ wo, uint16_t* __restrict__ out,
                                                   char* __restrict__ ws) {
  extern __shared__ __attribute__((aligned(256))) char smem[];
  constexpr int PROFOFF = LDS1 + 256; (void)PROFOFF;
  const int tid = threadIdx.x, lane = tid & 63;
  const int wv8 = __builtin_amdgcn_readfirstlane(tid >> 6);
  {
    uint64_t pc;
    asm volatile("s_getpc_b64 %0" : "=s"(pc));
    if (wv8 < 2) {
      const i32x4 srdc = make_srd((const void*)(pc & ~(uint64_t)255), CODE_BYTES);
      const unsigned sb = (unsigned)(uintptr_t)(__attribute__((address_space(3))) char*)smem;
      asm volatile("s_mov_b32 m0, %2\n\ts_nop 0\n\tbuffer_load_dword %0, %1, 0 offen lds"
                   :: "v"((unsigned)tid * 128u), "s"(srdc), "s"(sb + LDS1));
    }
  }
  STAMP_INIT(); W0(STAMP(0); CSTAMP(22));
  const int wv = wv8 & 3;
  const int bid = blockIdx.x, xcd = bid & 7, j = bid >> 3;
  constexpr int GN = 32 / GM, NGM = 32 / GM;
  const int mi = (xcd % NGM) * GM + (j % GM);
  const int ni = (xcd / NGM) * GN + (j / GM);
  const int b = mi >> 4, t0 = (mi & 15) * 64;
  const int mi2 = MAP2 ? xcd * 4 + (j & 3) : mi;
  const int cj = MAP2 ? (j >> 2) : ni;

  if (__builtin_expect(wv8 >= 4, 1)) {
    const int rot = ROT ? (((j % GM) * (GM >= 8 ? 1 : 8 / GM) + (j / GM) * (8 / (GN > 8 ? 8 : GN))) & 7) : 0;
    const i32x4 srdx = make_srd(x, 2 * T_LEN * DM * 2);
    const i32x4 srdw = make_srd(w, 3 * DM * 2 * DM * 2);
    const unsigned sbase = (unsigned)(uintptr_t)(__attribute__((address_space(3))) char*)smem;
    const unsigned wvoff = (unsigned)((lane >> 4) * 2048 + (((lane >> 3) ^ wv) & 1) * 1024 + ni * 128 + (((lane & 7) ^ (2 * (lane >> 4))) * 16));
    unsigned xvoff[3];
#pragma unroll
    for (int i = 0; i < 3; ++i) {
      int rho = i < 2 ? 16 * wv + 8 * i + (lane >> 3) : 64 + wv;
      int t = t0 - 4 + rho;
      int tc = t > T_LEN - 1 ? T_LEN - 1 : t;
      unsigned off = (unsigned)((b * T_LEN + tc) * 1024 + (((lane & 7) ^ xsw(rho >> 3)) * 16));
      xvoff[i] = t < 0 ? 0x80000000u : off;
    }
    auto issue = [&](int s, bool even, int wslot, int xslot) {
      const int cc = ((s >> 1) + rot) & 7;
      const int c0 = cc * 64 + (even ? 0 : 32);
      const unsigned wl = sbase + WOFF + wslot * WSLOT + 8 * wv * 256;
      const unsigned so = (unsigned)(c0 + 8 * wv) * 2048;
#pragma unroll
      for (int tp = 0; tp < 3; ++tp)
#pragma unroll
        for (int i = 0; i < 2; ++i)
          dma16(srdw, wvoff, so + (unsigned)((tp * 512 + 4 * i) * 2048), wl + tp * 8192 + 4 * i * 256);
      if (even) {
        const unsigned xl = sbase + XOFF + xslot * XSLOT;
        dma16(srdx, xvoff[0], cc * 128, xl + (16 * wv) * 128);
        dma16(srdx, xvoff[1], cc * 128, xl + (16 * wv + 8) * 128);
        dma16_lo8(srdx, xvoff[2], cc * 128, xl + (64 + wv) * 128);
      }
    };
    u32x2 ep;
    static_for<0, RING - 1>([&](auto S) { constexpr int s = S; issue(s, (s & 1) == 0, s % RING, (s >> 1) % XRING);
    });
    constexpr int MAIN = ((NS - RING + 1) / 2) * 2;
    int wsl = (RING - 1) % RING, xs = (((RING - 1) + 1) >> 1) % XRING;
    const i32x4 srdo = make_srd(wo, DM * DM * 2);
    const unsigned ovoff = (unsigned)((lane >> 3) * 1024 + cj * 128 +
                                      (((lane & 7) ^ (2 * (((lane >> 4) & 1) | ((wv & 1) << 1)))) * 16));
#pragma unroll 1
    for (int s = 0; s < MAIN; s += 2) {
      waitv<later(0)>();
      BARRIER();
      {
        constexpr bool ev = ((RING - 1) & 1) == 0;
        issue(s + RING - 1, ev, wsl, xs);
        wsl = wsl + 1 == RING ? 0 : wsl + 1;
        if (ev) xs = xs + 1 == XRING ? 0 : xs + 1;
      }
      waitv<later(1)>();
      BARRIER();
      {
        constexpr bool ev = (RING & 1) == 0;
        issue(s + RING, ev, wsl, xs);
        wsl = wsl + 1 == RING ? 0 : wsl + 1;
        if (ev) xs = xs + 1 == XRING ? 0 : xs + 1;
      }
    }
    static_for<MAIN, NS>([&](auto S) {
      constexpr int s = S;
      waitv<later(s)>();
      BARRIER();
      constexpr int p = s + RING - 1 - NS;
      if constexpr (p == 0)
        asm volatile("global_load_dwordx2 %0, %1, off sc1" : "=v"(ep) : "v"(ws + EPOCH_OFF) : "memory");
      if constexpr (p >= 0 && p < 3) {
#pragma unroll
        for (int i = 6 * p; i < (p == 2 ? 16 : 6 * p + 6); ++i) {
          const int blk = 4 * i + wv;
          dma16(srdo, ovoff, (unsigned)(blk * 8192), sbase + WO_OFF + blk * 1024);
        }
      }
    });
    waitv<0>();
    asm volatile("" : "+v"(ep));
    BARRIER();
    u32x2 tag = {ep[0] + 1u, ep[1]};
    if (wv8 == 4) {
      if (lane == 0) store8_dev(ws + FLAG_OFF + (mi * 8 + ni) * 128, tag);
      const char* fp = ws + FLAG_OFF + (mi2 * 8 + (lane & 7)) * 128;
      for (int it = 0; it < SPIN_MAX; ++it) {
        u32x2 v = load8_dev(fp);
        if (__builtin_amdgcn_ballot_w64(v[0] != tag[0] || v[1] != tag[1]) == 0) break;
        __builtin_amdgcn_s_sleep(1);
      }
      STAMP(20);
      BARRIER();
    } else {
      BARRIER();
      if (wv8 == 5 && bid == 0) {
        for (int it = 0; it < SPIN_MAX; ++it) {
          bool bad = false;
#pragma unroll
          for (int i = 0; i < 4; ++i) {
            u32x2 v = load8_dev(ws + FLAG_OFF + (i * 64 + lane) * 128);
            bad = bad || v[0] != tag[0] || v[1] != tag[1];
          }
          if (__builtin_amdgcn_ballot_w64(bad) == 0) break;
          __builtin_amdgcn_s_sleep(1);
        }
        if (lane == 0) store8_dev(ws + EPOCH_OFF, tag);
      }
    }
    return;
  }

  const int G = lane >> 4, q4 = (lane >> 2) & 3, p4 = lane & 3;
  const unsigned wrdf = (unsigned)(WOFF + (8 * G + q4) * 256 + (G & 1) * 128 + ((wv ^ q4) * 32) + p4 * 8);
  const unsigned wrdg = wrdf ^ 128u;
  const int n16 = lane & 15, xq = n16 >> 1, xe = n16 & 1;
  const unsigned xrd0 = (unsigned)(XOFF + (8 * xq + xe) * 128 + ((G ^ xsw(xq)) * 16));
  const unsigned xrd1 = (unsigned)(XOFF + (8 * xq + xe) * 128 + ((G ^ xsw(xq + 1)) * 16));

  f32x4 af[4] = {{0}, {0}, {0}, {0}}, ag[4] = {{0}, {0}, {0}, {0}};
  auto group = [&](auto TP, const Frags& U, auto&& ld) {
    constexpr int tp = TP;
    static_for<0, 4>([&](auto Bk) {
      constexpr int bk = Bk;
      MFMA16_ACC(af[bk], U.wf[tp], U.x[bk + 2 - tp]);
      ld(std::integral_constant<int, 2 * bk>{});
      SB();
      MFMA16_ACC(ag[bk], U.wg[tp], U.x[bk + 2 - tp]);
      ld(std::integral_constant<int, 2 * bk + 1>{});
      SB();
    });
  };
  using I0 = std::integral_constant<int, 0>;
  using I1 = std::integral_constant<int, 1>;
  using I2 = std::integral_constant<int, 2>;
  auto stage = [&](Frags& L, const Frags& U, unsigned pf, unsigned pg, unsigned xa, unsigned xb) {
    auto ldx = [&](int i) { L.x[i] = *(lds_bf16x8)(uintptr_t)((i < 4 ? xa : xb) + i * 256); };
    auto ld_a = [&](auto I) {
      constexpr int i = I;
      if constexpr (i == 0) L.wf[0] = tr_frag_l(pf, 1024);
      if constexpr (i == 1) ldx(2);
      if constexpr (i == 2) L.wg[0] = tr_frag_l(pg, 1024);
      if constexpr (i == 3) ldx(3);
      if constexpr (i == 4) ldx(4);
      if constexpr (i == 5) ldx(5);
    };
    auto ld_b = [&](auto I) {
      constexpr int i = I;
      if constexpr (i == 0) L.wf[1] = tr_frag_l(pf + 8192, 1024);
      if constexpr (i == 1) ldx(1);
      if constexpr (i == 2) L.wg[1] = tr_frag_l(pg + 8192, 1024);
    };
    auto ld_c = [&](auto I) {
      constexpr int i = I;
      if constexpr (i == 0) L.wf[2] = tr_frag_l(pf + 16384, 1024);
      if constexpr (i == 1) ldx(0);
      if constexpr (i == 2) L.wg[2] = tr_frag_l(pg + 16384, 1024);
    };
    auto ld_bc = [&](auto I) {
      constexpr int i = I;
      if constexpr (i < 3) ld_b(I); else ld_c(std::integral_constant<int, i - 3>{});
    };
    auto ld_0 = [&](auto) {};
    if constexpr (LAG == 2) {
      group(I1{}, U, ld_a);
      group(I2{}, U, ld_bc);
      group(I0{}, L, ld_0);
    } else {
      group(I2{}, U, ld_a);
      group(I0{}, L, ld_b);
      group(I1{}, L, ld_c);
    }
  };
  Frags A, B;
  {
    const bf16x8 zero = {0};
#pragma unroll
    for (int i = 0; i < 6; ++i) B.x[i] = zero;
#pragma unroll
    for (int i = 0; i < 3; ++i) B.wf[i] = B.wg[i] = zero;
  }
  int wsl = 0, xsl = 0;
  const unsigned sb0 = (unsigned)(uintptr_t)(__attribute__((address_space(3))) char*)smem;
#pragma unroll 1
  for (int s = 0; s < NS; s += 2) {
    const unsigned xbase = sb0 + xsl * XSLOT;
    unsigned pf = sb0 + wsl * WSLOT + wrdf, pg = sb0 + wsl * WSLOT + wrdg;
    unsigned xa = xbase + xrd0, xb = xbase + xrd1;
    asm volatile("" : "+v"(pf), "+v"(pg), "+v"(xa), "+v"(xb));
    CBARRIER();
    stage(A, B, pf, pg, xa, xb);
    wsl = wsl + 1 == RING ? 0 : wsl + 1;
    pf = sb0 + wsl * WSLOT + wrdf; pg = sb0 + wsl * WSLOT + wrdg;
    xa = xbase + (xrd0 ^ 64u); xb = xbase + (xrd1 ^ 64u);
    asm volatile("" : "+v"(pf), "+v"(pg), "+v"(xa), "+v"(xb));
    CBARRIER();
    stage(B, A, pf, pg, xa, xb);
    wsl = wsl + 1 == RING ? 0 : wsl + 1;
    xsl = xsl + 1 == XRING ? 0 : xsl + 1;
  }
  {
    auto ld_0 = [&](auto) {};
    if constexpr (LAG == 2) group(I1{}, B, ld_0);
    group(I2{}, B, ld_0);
    asm volatile("s_nop 7\n\ts_nop 7" ::: "memory");
    SB();
  }
  W0(STAMP(3); CSTAMP(17));

  const __amdgpu_buffer_rsrc_t zr = __builtin_amdgcn_make_buffer_rsrc(ws, 0, ZBYTES, 0x00020000);
  {
    const int kg = ni * 4 + wv;
    const unsigned zo = (unsigned)((((mi * 4 + (G & 1)) * 16 + (kg >> 1)) * 64 + ((kg & 1) * 2 + (G >> 1)) * 16 + n16) * 16);
#pragma unroll
    for (int m = 0; m < 2; ++m) {
      const f32x4 &f0 = af[2 * m], &g0 = ag[2 * m], &f1 = af[2 * m + 1], &g1 = ag[2 * m + 1];
      const auto s0 = __builtin_amdgcn_permlane16_swap(pack_bf16(gate(f0[0], g0[0]), gate(f0[1], g0[1])),
                                                       pack_bf16(gate(f1[0], g1[0]), gate(f1[1], g1[1])), false, false);
      const auto s1 = __builtin_amdgcn_permlane16_swap(pack_bf16(gate(f0[2], g0[2]), gate(f0[3], g0[3])),
                                                       pack_bf16(gate(f1[2], g1[2]), gate(f1[3], g1[3])), false, false);
      const u32x4 o = {s0[0], s1[0], s0[1], s1[1]};
      __builtin_amdgcn_raw_buffer_store_b128(o, zr, zo + m * 2 * 16 * 1024, 0, 16);
    }
  }
  W0(STAMP(4));
  waitv<0>();
  W0(STAMP(5));
  BARRIER();
  W0(STAMP(6); CSTAMP(19));

  const int f2 = ((q4 >> 1) & 1) | ((G & 1) << 1);
  const unsigned ard = (unsigned)(WO_OFF + (8 * G + q4) * 128 + f2 * 32 + p4 * 8);
  bf16x8 wa[8][4];
#pragma unroll
  for (int ks = 0; ks < 8; ++ks)
#pragma unroll
    for (int ct = 0; ct < 4; ++ct) wa[ks][ct] = tr_frag(smem + (ard ^ (unsigned)(ct * 32)) + ks * 4096, 512);
  BARRIER();
  W0(STAMP(7));
  u32x4 z[16];
  {
    const unsigned zo = (unsigned)((((mi2 * 4 + wv) * 16) * 64 + lane) * 16);
#pragma unroll
    for (int ks = 0; ks < 16; ++ks) z[ks] = __builtin_amdgcn_raw_buffer_load_b128(zr, zo, ks * 1024, 16);
  }
  SB();
  f32x4 acc[4] = {{0}, {0}, {0}, {0}};
#pragma unroll
  for (int ks = 0; ks < 8; ++ks) {
#pragma unroll
    for (int ct = 0; ct < 4; ++ct) acc[ct] = MFMA16(wa[ks][ct], __builtin_bit_cast(bf16x8, z[ks]), acc[ct]);
#pragma unroll
    for (int ct = 0; ct < 4; ++ct) wa[ks][ct] = tr_frag(smem + (ard ^ (unsigned)(ct * 32)) + (ks + 8) * 4096, 512);
    SB();
  }
  W0(STAMP(8));
#pragma unroll
  for (int ks = 0; ks < 8; ++ks) {
#pragma unroll
    for (int ct = 0; ct < 4; ++ct) acc[ct] = MFMA16(wa[ks][ct], __builtin_bit_cast(bf16x8, z[ks + 8]), acc[ct]);
    SB();
  }
  W0(STAMP(9));
  {
    const int t = (mi2 & 15) * 64 + 8 * xq + 2 * wv + xe;
    if (t < T_LEN) {
      uint16_t* op = out + (size_t)((mi2 >> 4) * T_LEN + t) * DM + cj * 64 + 4 * G;
#pragma unroll
      for (int ct = 0; ct < 4; ++ct) {
        u32x2 o = {pack_bf16(acc[ct][0], acc[ct][1]), pack_bf16(acc[ct][2], acc[ct][3])};
        *(u32x2*)(op + ct * 16) = o;
      }
    }
  }
  W0(STAMP(18); CSTAMP(23));
}

extern "C" void kernel_launch(void* const* d_in, const int* in_sizes, int n_in, void* d_out, int out_size,
                              void* d_ws, size_t ws_size, hipStream_t stream) {
  const uint16_t* x = (const uint16_t*)d_in[0];
  const uint16_t* w = (const uint16_t*)d_in[1];
  const uint16_t* wo = (const uint16_t*)d_in[2];
  (void)hipFuncSetAttribute((const void*)wn_fused, hipFuncAttributeMaxDynamicSharedMemorySize, LDS1 + 256 + PROF_LDS);
  wn_fused<<<256, 512, LDS1 + 256 + PROF_LDS, stream>>>(x, w, wo, (uint16_t*)d_out, (char*)d_ws);
}
```

```cpp
#include <hip/hip_runtime.h>
#include <stdint.h>
#include <type_traits>

typedef short s16x4 __attribute__((ext_vector_type(4)));
typedef short s16x8 __attribute__((ext_vector_type(8)));
typedef __bf16 bf16x8 __attribute__((ext_vector_type(8)));
typedef __bf16 bf16x2 __attribute__((ext_vector_type(2)));
typedef float f32x16 __attribute__((ext_vector_type(16)));
typedef float f32x4 __attribute__((ext_vector_type(4)));
typedef float f32x2 __attribute__((ext_vector_type(2)));
typedef int i32x4 __attribute__((ext_vector_type(4)));
typedef unsigned u32x4 __attribute__((ext_vector_type(4)));
typedef unsigned u32x2 __attribute__((ext_vector_type(2)));
typedef __attribute__((address_space(3))) s16x4* lds_s16x4;

#define RING 5
#define GM 16
#define CODE_BYTES 9216
#define OUTAUX 16
#define LEAD 3
#define PSLEEP "3"

constexpr int T_LEN = 1000, DM = 512;
constexpr int WSLOT = 24576;
constexpr int XSLOT = 68 * 128;
constexpr int XRING = (RING + 2) / 2;
constexpr int XOFF = 0;
constexpr int WOFF = ((XRING * XSLOT + 255) / 256) * 256;
constexpr int LDS1 = WOFF + RING * WSLOT;
constexpr int WO_OFF = WOFF + WSLOT;
constexpr int NS = 16;
constexpr unsigned ZBYTES = 32u * 64u * 512u * 2u;
constexpr unsigned FLAG_OFF = ZBYTES;
constexpr unsigned EPOCH_OFF = ZBYTES + 256u * 128u;
constexpr int SPIN_MAX = 1 << 18;
constexpr int SYNC_OFF = LDS1 + 256;
constexpr int LDS_TOTAL = LDS1 + 512;
constexpr int S0 = 1;

__device__ __forceinline__ i32x4 make_srd(const void* p, unsigned bytes) {
  uint64_t a = (uint64_t)p;
  i32x4 r;
  r[0] = (int)(unsigned)a;
  r[1] = (int)(unsigned)(a >> 32);
  r[2] = (int)bytes;
  r[3] = 0x00020000;
  return r;
}

__device__ __forceinline__ void dma16(i32x4 srd, unsigned voff, unsigned soff, unsigned ldsaddr) {
  asm volatile("s_mov_b32 m0, %3\n\ts_nop 0\n\tbuffer_load_dwordx4 %0, %1, %2 offen lds"
               :: "v"(voff), "s"(srd), "s"(soff), "s"(ldsaddr));
}
__device__ __forceinline__ void dma16_lo8(i32x4 srd, unsigned voff, unsigned soff, unsigned ldsaddr) {
  asm volatile("s_mov_b32 m0, %3\n\ts_mov_b64 exec, 0xff\n\tbuffer_load_dwordx4 %0, %1, %2 offen lds\n\ts_mov_b64 exec, -1"
               :: "v"(voff), "s"(srd), "s"(soff), "s"(ldsaddr));
}
template <int N> __device__ __forceinline__ void waitv() { asm volatile("s_waitcnt vmcnt(%0)" :: "i"(N) : "memory"); }
#define BARRIER() asm volatile("s_waitcnt lgkmcnt(0)\n\ts_barrier" ::: "memory")
#define SB() __builtin_amdgcn_sched_barrier(0)

constexpr int stage_ops(int u) { return u < NS ? ((u & 1) ? 6 : 9) : (u == NS ? 7 : (u == NS + 1 ? 6 : (u == NS + 2 ? 4 : 0))); }
constexpr int later(int s) {
  int n = 0;
  for (int d = 1; d <= LEAD - 1; ++d) n += stage_ops(s + d);
  return n;
}

__device__ __forceinline__ unsigned pack_bf16(float a, float b) {
  f32x2 v = {a, b};
  bf16x2 r = __builtin_convertvector(v, bf16x2);
  return __builtin_bit_cast(unsigned, r);
}

__device__ __forceinline__ unsigned gate2(f32x2 f, f32x2 g) {
  const float L2E = 1.4426950408889634f;
  const f32x2 fa = f * (-2.0f * L2E), ga = g * (-L2E);
  const f32x2 u = {__builtin_amdgcn_exp2f(fminf(fa[0], 60.0f)), __builtin_amdgcn_exp2f(fminf(fa[1], 60.0f))};
  const f32x2 b = {__builtin_amdgcn_exp2f(ga[0]), __builtin_amdgcn_exp2f(ga[1])};
  const f32x2 t1 = u + 1.0f;
  const f32x2 den = __builtin_elementwise_fma(t1, b, t1);
  const f32x2 r = {__builtin_amdgcn_rcpf(den[0]), __builtin_amdgcn_rcpf(den[1])};
  const f32x2 z = (2.0f - t1) * r;
  return __builtin_bit_cast(unsigned, __builtin_convertvector(z, bf16x2));
}

__device__ __forceinline__ bf16x8 tr_frag(const char* p, int step) {
  s16x4 lo = __builtin_amdgcn_ds_read_tr16_b64_v4i16((lds_s16x4)(p));
  s16x4 hi = __builtin_amdgcn_ds_read_tr16_b64_v4i16((lds_s16x4)(p + step));
  s16x8 v = __builtin_shufflevector(lo, hi, 0, 1, 2, 3, 4, 5, 6, 7);
  return __builtin_bit_cast(bf16x8, v);
}

struct Frags { bf16x8 x0, x1, x2, x3, w0, w1, w2; };
#define MFMA(a, b, c) __builtin_amdgcn_mfma_f32_32x32x16_bf16(a, b, c, 0, 0, 0)
#define MFMA16(a, b, c) __builtin_amdgcn_mfma_f32_16x16x32_bf16(a, b, c, 0, 0, 0)

template <int S, int E, class F> __device__ __forceinline__ void static_for(F&& f) {
  if constexpr (S < E) { f(std::integral_constant<int, S>{}); static_for<S + 1, E>(f); }
}

__device__ __forceinline__ u32x2 load8_dev(const char* p) {
  u32x2 v;
  asm volatile("global_load_dwordx2 %0, %1, off sc1\n\ts_waitcnt vmcnt(0)" : "=v"(v) : "v"(p) : "memory");
  return v;
}
__device__ __forceinline__ void store8_dev(char* p, u32x2 v) {
  asm volatile("global_store_dwordx2 %0, %1, off sc1" :: "v"(p), "v"(v) : "memory");
}

__global__ __launch_bounds__(512, 2) void wn_fused(const uint16_t* __restrict__ x, const uint16_t* __restrict__ w,
                                                   const uint16_t* __restrict__ wo, uint16_t* __restrict__ out,
                                                   char* __restrict__ ws) {
  extern __shared__ __attribute__((aligned(256))) char smem[];
  const int tid = threadIdx.x, lane = tid & 63;
  const int wv8 = __builtin_amdgcn_readfirstlane(tid >> 6);
  {
    uint64_t pc;
    asm volatile("s_getpc_b64 %0" : "=s"(pc));
    const i32x4 srdc = make_srd((const void*)(pc & ~(uint64_t)255), CODE_BYTES);
    const unsigned sb = (unsigned)(uintptr_t)(__attribute__((address_space(3))) char*)smem;
    asm volatile("s_mov_b32 m0, %2\n\ts_nop 0\n\tbuffer_load_dword %0, %1, 0 offen lds"
                 :: "v"((unsigned)tid * 128u), "s"(srdc), "s"(sb + LDS1));
    SB();
  }
  const int wv = wv8 & 3;
  const int bid = blockIdx.x, xcd = bid & 7, j = bid >> 3;
  constexpr int GN = 32 / GM, NGM = 32 / GM;
  const int mi = (xcd % NGM) * GM + (j % GM);
  const int ni = (xcd / NGM) * GN + (j / GM);
  const int b = mi >> 4, t0 = (mi & 15) * 64;
  const int mi2 = xcd * 4 + (j & 3);
  const int cj = j >> 2;

  const int rot = ((j % GM) + (j / GM) * 4) & 7;
  const i32x4 srdx = make_srd(x, 2 * T_LEN * DM * 2);
  const i32x4 srdw = make_srd(w, 3 * DM * 2 * DM * 2);
  const unsigned sbase = (unsigned)(uintptr_t)(__attribute__((address_space(3))) char*)smem;
  const unsigned wvoff = (unsigned)((lane >> 4) * 2048 + ((lane >> 3) & 1) * 1024 + ni * 128 + (((lane & 7) ^ (2 * (lane >> 4))) * 16));
  unsigned xvoff[3];
#pragma unroll
  for (int i = 0; i < 3; ++i) {
    int rho = i < 2 ? 16 * wv + 8 * i + (lane >> 3) : 64 + wv;
    int t = t0 - 4 + rho;
    int tc = t > T_LEN - 1 ? T_LEN - 1 : t;
    unsigned off = (unsigned)((b * T_LEN + tc) * 1024 + (((lane & 7) ^ ((rho >> 2) & 7)) * 16));
    xvoff[i] = t < 0 ? 0x80000000u : off;
  }
  auto issue = [&](int s, bool even, int wslot, int xslot) {
    const int cc = ((s >> 1) + rot) & 7;
    const int c0 = cc * 64 + (even ? 0 : 32);
    const unsigned wl = sbase + WOFF + wslot * WSLOT + 8 * wv * 256;
    const unsigned so = (unsigned)(c0 + 8 * wv) * 2048;
#pragma unroll
    for (int tp = 0; tp < 3; ++tp)
#pragma unroll
      for (int i = 0; i < 2; ++i)
        dma16(srdw, wvoff, so + (unsigned)((tp * 512 + 4 * i) * 2048), wl + tp * 8192 + 4 * i * 256);
    if (even) {
      const unsigned xl = sbase + XOFF + xslot * XSLOT;
      dma16(srdx, xvoff[0], cc * 128, xl + (16 * wv) * 128);
      dma16(srdx, xvoff[1], cc * 128, xl + (16 * wv + 8) * 128);
      dma16_lo8(srdx, xvoff[2], cc * 128, xl + (64 + wv) * 128);
    }
  };

  volatile __attribute__((address_space(3))) unsigned* fl = (volatile __attribute__((address_space(3))) unsigned*)(smem + SYNC_OFF);
  if (wv8 < 4) {
    issue(0, true, 0, 0);
    if (tid < 4) fl[tid] = 0;
  }
  BARRIER();
  if (__builtin_expect(wv8 >= 4, 1)) {
    u32x2 ep;
    static_for<S0, S0 + LEAD>([&](auto S) { constexpr int s = S; issue(s, (s & 1) == 0, s % RING, (s >> 1) % XRING);
    });
    constexpr int MAIN = ((NS - LEAD - S0) / 2) * 2;
    int wsl = (S0 + LEAD) % RING, xs = ((S0 + LEAD + 1) >> 1) % XRING;
    const i32x4 srdo = make_srd(wo, DM * DM * 2);
    const unsigned ovoff = (unsigned)((lane >> 3) * 1024 + cj * 128 +
                                      (((lane & 7) ^ (2 * (((lane >> 4) & 1) | ((wv & 1) << 1)))) * 16));
#pragma unroll 1
    for (int s = S0; s < S0 + MAIN; s += 2) {
      waitv<later(S0)>();
      BARRIER();
      {
        constexpr bool ev = ((S0 + LEAD) & 1) == 0;
        issue(s + LEAD, ev, wsl, xs);
        wsl = wsl + 1 == RING ? 0 : wsl + 1;
        if (ev) xs = xs + 1 == XRING ? 0 : xs + 1;
      }
      waitv<later(S0 + 1)>();
      BARRIER();
      {
        constexpr bool ev = ((S0 + LEAD + 1) & 1) == 0;
        issue(s + LEAD + 1, ev, wsl, xs);
        wsl = wsl + 1 == RING ? 0 : wsl + 1;
        if (ev) xs = xs + 1 == XRING ? 0 : xs + 1;
      }
    }
    static_for<S0 + MAIN, NS>([&](auto S) {
      constexpr int s = S;
      waitv<later(s)>();
      BARRIER();
      constexpr int p = s + LEAD - NS;
      if constexpr (p < 0) issue(s + LEAD, ((s + LEAD) & 1) == 0, (s + LEAD) % RING, ((s + LEAD) >> 1) % XRING);
      if constexpr (p == 0)
        asm volatile("global_load_dwordx2 %0, %1, off sc1" : "=v"(ep) : "v"(ws + EPOCH_OFF) : "memory");
      if constexpr (p >= 0 && p < 3) {
#pragma unroll
        for (int i = 6 * p; i < (p == 2 ? 16 : 6 * p + 6); ++i) {
          const int blk = 4 * i + wv;
          dma16(srdo, ovoff, (unsigned)(blk * 8192), sbase + WO_OFF + blk * 1024);
        }
      }
    });
    waitv<0>();
    asm volatile("" : "+v"(ep));
    BARRIER();
    u32x2 tag = {ep[0] + 1u, ep[1]};
    if (wv8 == 4) {
      if (lane == 0) store8_dev(ws + FLAG_OFF + (mi * 8 + ni) * 128, tag);
      const char* fp = ws + FLAG_OFF + (mi2 * 8 + (lane & 7)) * 128;
      {
        u32x2 p0, p1, p2, p3;
        int cnt = SPIN_MAX;
        asm volatile(
            "global_load_dwordx2 %0, %5, off sc1\n\ts_sleep " PSLEEP "\n\t"
            "global_load_dwordx2 %1, %5, off sc1\n\ts_sleep " PSLEEP "\n\t"
            "global_load_dwordx2 %2, %5, off sc1\n\ts_sleep " PSLEEP "\n\t"
            "global_load_dwordx2 %3, %5, off sc1\n"
            "1:\n\t"
            "s_waitcnt vmcnt(3)\n\tv_cmp_ne_u64 vcc, %0, %6\n\ts_cbranch_vccz 2f\n\tglobal_load_dwordx2 %0, %5, off sc1\n\t"
            "s_waitcnt vmcnt(3)\n\tv_cmp_ne_u64 vcc, %1, %6\n\ts_cbranch_vccz 2f\n\tglobal_load_dwordx2 %1, %5, off sc1\n\t"
            "s_waitcnt vmcnt(3)\n\tv_cmp_ne_u64 vcc, %2, %6\n\ts_cbranch_vccz 2f\n\tglobal_load_dwordx2 %2, %5, off sc1\n\t"
            "s_waitcnt vmcnt(3)\n\tv_cmp_ne_u64 vcc, %3, %6\n\ts_cbranch_vccz 2f\n\tglobal_load_dwordx2 %3, %5, off sc1\n\t"
            "s_sub_u32 %4, %4, 1\n\ts_cmp_lg_u32 %4, 0\n\ts_cbranch_scc1 1b\n"
            "2:\n\t"
            "s_barrier\n\ts_waitcnt vmcnt(0)"
            : "=&v"(p0), "=&v"(p1), "=&v"(p2), "=&v"(p3), "+s"(cnt)
            : "v"(fp), "v"(tag)
            : "vcc", "scc", "memory");
      }
    } else {
      BARRIER();
      if (wv8 == 5 && bid == 0) {
        for (int it = 0; it < SPIN_MAX; ++it) {
          bool bad = false;
#pragma unroll
          for (int i = 0; i < 4; ++i) {
            u32x2 v = load8_dev(ws + FLAG_OFF + (i * 64 + lane) * 128);
            bad = bad || v[0] != tag[0] || v[1] != tag[1];
          }
          if (__builtin_amdgcn_ballot_w64(bad) == 0) break;
          __builtin_amdgcn_s_sleep(1);
        }
        if (lane == 0) store8_dev(ws + EPOCH_OFF, tag);
      }
    }
    return;
  }

  auto arrive = [&](int k) {
    if (lane == 0) fl[4 * k + wv] = 1;
    for (;;) {
      const unsigned a0 = fl[4 * k], a1 = fl[4 * k + 1], a2 = fl[4 * k + 2], a3 = fl[4 * k + 3];
      if (a0 & a1 & a2 & a3) break;
    }
    asm volatile("" ::: "memory");
  };
  waitv<0>();
  arrive(0);
  const int G = lane >> 4, q4 = (lane >> 2) & 3, p4 = lane & 3;
  const int pperm = ((p4 & 1) << 1) | (p4 >> 1);
  const unsigned wrd = (unsigned)(WOFF + (8 * (G >> 1) + q4) * 256 + (G & 1) * 128 + ((wv ^ q4) * 32) + pperm * 8);
  const int c32 = lane & 31, h = lane >> 5, xq = c32 >> 1, xe = c32 & 1;
  const unsigned xrd0 = (unsigned)(XOFF + (4 * xq + xe) * 128 + ((h ^ (xq & 7)) * 16));
  const unsigned xrd1 = (unsigned)(XOFF + (4 * (xq + 1) + xe) * 128 + ((h ^ ((xq + 1) & 7)) * 16));

  f32x16 acc0 = {0}, acc1 = {0};
  auto half = [&](Frags& L, const Frags& U, const char* wp, const char* xa, const char* xb) {
    acc0 = MFMA(U.w0, U.x2, acc0);
    L.w0 = tr_frag(wp, 1024); L.x2 = *(const bf16x8*)(xb);
    SB();
    acc1 = MFMA(U.w0, U.x3, acc1);
    L.x3 = *(const bf16x8*)(xb + 256); L.w1 = tr_frag(wp + 8192, 1024);
    SB();
    acc0 = MFMA(U.w1, U.x1, acc0);
    L.x1 = *(const bf16x8*)(xa + 256); L.w2 = tr_frag(wp + 16384, 1024);
    SB();
    acc1 = MFMA(U.w1, U.x2, acc1);
    L.x0 = *(const bf16x8*)(xa);
    SB();
    acc0 = MFMA(U.w2, U.x0, acc0);
    SB();
    acc1 = MFMA(U.w2, U.x1, acc1);
    SB();
  };
  Frags A, B;
  {
    const bf16x8 zero = {0};
    B.x0 = B.x1 = B.x2 = B.x3 = B.w0 = B.w1 = B.w2 = zero;
  }
  int wsl = 0, xsl = 0;
#pragma unroll 1
  for (int s = 0; s < NS; s += 2) {
    const char* xbase = smem + xsl * XSLOT;
#pragma unroll
    for (int o = 0; o < 2; ++o) {
      if (o || s) BARRIER();
      const char* wp = smem + wsl * WSLOT + wrd;
      half(A, B, wp, xbase + (xrd0 ^ (unsigned)(o * 64)), xbase + (xrd1 ^ (unsigned)(o * 64)));
      half(B, A, wp + 16 * 256, xbase + (xrd0 ^ (unsigned)(o * 64 + 32)), xbase + (xrd1 ^ (unsigned)(o * 64 + 32)));
      wsl = wsl + 1 == RING ? 0 : wsl + 1;
    }
    xsl = xsl + 1 == XRING ? 0 : xsl + 1;
  }
  acc0 = MFMA(B.w0, B.x2, acc0);
  acc0 = MFMA(B.w1, B.x1, acc0);
  acc0 = MFMA(B.w2, B.x0, acc0);
  SB();

  const __amdgpu_buffer_rsrc_t zr = __builtin_amdgcn_make_buffer_rsrc(ws, 0, ZBYTES, 0x00020000);
  {
    const int kg = ni * 4 + wv;
    const unsigned zo = (unsigned)((((mi * 4 + (c32 >> 4)) * 16 + (kg >> 1)) * 64 + ((kg & 1) * 2 + h) * 16 + (c32 & 15)) * 16);
    auto g2 = [&](const f32x16& a, int r) { return gate2(f32x2{a[2 * r], a[2 * r + 1]}, f32x2{a[2 * r + 8], a[2 * r + 9]}); };
    u32x4 o;
    acc1 = MFMA(B.w0, B.x3, acc1);
    acc1 = MFMA(B.w1, B.x2, acc1);
    acc1 = MFMA(B.w2, B.x1, acc1);
#pragma unroll
    for (int r = 0; r < 4; ++r) o[r] = g2(acc0, r);
    __builtin_amdgcn_raw_buffer_store_b128(o, zr, zo, 0, 16);
    __builtin_amdgcn_sched_group_barrier(0x008, 1, 0);
    __builtin_amdgcn_sched_group_barrier(0x402, 10, 0);
    __builtin_amdgcn_sched_group_barrier(0x008, 1, 0);
    __builtin_amdgcn_sched_group_barrier(0x402, 8, 0);
    __builtin_amdgcn_sched_group_barrier(0x008, 1, 0);
    SB();
#pragma unroll
    for (int r = 0; r < 4; ++r) o[r] = g2(acc1, r);
    __builtin_amdgcn_raw_buffer_store_b128(o, zr, zo + 2 * 16 * 1024, 0, 16);
  }
  waitv<0>();
  BARRIER();

  const int f2 = ((q4 >> 1) & 1) | ((G & 1) << 1);
  const unsigned ard = (unsigned)(WO_OFF + (8 * G + q4) * 128 + f2 * 32 + p4 * 8);
  bf16x8 wa[8][4];
#pragma unroll
  for (int ks = 0; ks < 8; ++ks)
#pragma unroll
    for (int ct = 0; ct < 4; ++ct) wa[ks][ct] = tr_frag(smem + (ard ^ (unsigned)(ct * 32)) + ks * 4096, 512);
  BARRIER();
  u32x4 z[16];
  {
    const unsigned zo = (unsigned)((((mi2 * 4 + wv) * 16) * 64 + lane) * 16);
#pragma unroll
    for (int ks = 0; ks < 16; ++ks) z[ks] = __builtin_amdgcn_raw_buffer_load_b128(zr, zo, ks * 1024, 16);
  }
  SB();
  f32x4 acc[4] = {{0}, {0}, {0}, {0}};
#pragma unroll
  for (int ks = 0; ks < 8; ++ks) {
#pragma unroll
    for (int ct = 0; ct < 4; ++ct) acc[ct] = MFMA16(wa[ks][ct], __builtin_bit_cast(bf16x8, z[ks]), acc[ct]);
#pragma unroll
    for (int ct = 0; ct < 4; ++ct) wa[ks][ct] = tr_frag(smem + (ard ^ (unsigned)(ct * 32)) + (ks + 8) * 4096, 512);
    SB();
  }
#pragma unroll
  for (int ks = 0; ks < 8; ++ks) {
#pragma unroll
    for (int ct = 0; ct < 4; ++ct) acc[ct] = MFMA16(wa[ks][ct], __builtin_bit_cast(bf16x8, z[ks + 8]), acc[ct]);
    SB();
  }
  {
    const int cc = 16 * (wv & 1) + (lane & 15);
    const int t = (mi2 & 15) * 64 + 4 * (cc >> 1) + (cc & 1) + 2 * (wv >> 1);
    const __amdgpu_buffer_rsrc_t orr = __builtin_amdgcn_make_buffer_rsrc(out, 0, 2 * T_LEN * DM * 2, 0x00020000);
    const unsigned oo = t < T_LEN ? (unsigned)((((mi2 >> 4) * T_LEN + t) * DM + cj * 64 + (G & 1) * 16 + (G >> 1) * 8) * 2) : 0x80000000u;
#pragma unroll
    for (int cp = 0; cp < 2; ++cp) {
      const unsigned a0 = pack_bf16(acc[2 * cp][0], acc[2 * cp][1]), a1 = pack_bf16(acc[2 * cp][2], acc[2 * cp][3]);
      const unsigned b0 = pack_bf16(acc[2 * cp + 1][0], acc[2 * cp + 1][1]), b1 = pack_bf16(acc[2 * cp + 1][2], acc[2 * cp + 1][3]);
      const u32x2 s0 = __builtin_amdgcn_permlane16_swap(a0, b0, false, false);
      const u32x2 s1 = __builtin_amdgcn_permlane16_swap(a1, b1, false, false);
      const u32x4 o = {s0[0], s1[0], s0[1], s1[1]};
      __builtin_amdgcn_raw_buffer_store_b128(o, orr, oo + cp * 64, 0, OUTAUX);
    }
  }
}

extern "C" void kernel_launch(void* const* d_in, const int* in_sizes, int n_in, void* d_out, int out_size,
                              void* d_ws, size_t ws_size, hipStream_t stream) {
  const uint16_t* x = (const uint16_t*)d_in[0];
  const uint16_t* w = (const uint16_t*)d_in[1];
  const uint16_t* wo = (const uint16_t*)d_in[2];
  (void)hipFuncSetAttribute((const void*)wn_fused, hipFuncAttributeMaxDynamicSharedMemorySize, LDS_TOTAL);
  wn_fused<<<256, 512, LDS_TOTAL, stream>>>(x, w, wo, (uint16_t*)d_out, (char*)d_ws);
}
```
